```python
import torch

def _softmax(x, axis=-1):
    return torch.softmax(x, dim=axis)
def _gelu(x):
    return torch.nn.functional.gelu(x, approximate="tanh")
def _silu(x):
    return torch.nn.functional.silu(x)
def _sigmoid(x):
    return torch.sigmoid(x)
def _layernorm(x, axis=-1, eps=1e-5):
    m = x.mean(dim=axis, keepdim=True)
    v = x.var(dim=axis, keepdim=True, unbiased=False)
    return (x - m) / torch.sqrt(v + eps)
def _scatter_add(tgt, idx, src, axis=0):
    return tgt.clone().index_add_(axis, idx, src)
def _segment_sum(src, seg, n):
    out = torch.zeros((n,) + src.shape[1:], dtype=src.dtype)
    return out.index_add_(0, seg, src)
def _pad(x, pw):
    flat = tuple(v for pair in reversed(pw) for v in pair)
    return torch.nn.functional.pad(x, flat)

kernel_name = 'gated_mix__grouped_kv_mqa__moe_arch_mod__prompt'

def setup_inputs():

    _w = (lambda a: (lambda t: t.to(torch.bfloat16) if t.is_floating_point() else t)(torch.as_tensor(a)))
    def _A_setup():
        B, S, D, H = 1, 2048, 640, 32
        Dh = D // H
        return {
            "x": _w((lambda *s: torch.randn(*s, dtype=torch.bfloat16) * 0.05)(B, S, D)),
            "wq": _w((lambda *s: torch.randn(*s, dtype=torch.bfloat16) * 0.8)(D, H * Dh)),
            "wk": _w((lambda *s: torch.randn(*s, dtype=torch.bfloat16) * 0.8)(D, Dh)),
            "wv": _w((lambda *s: torch.randn(*s, dtype=torch.bfloat16) * 0.2)(D, Dh)),
            "wo": _w((lambda *s: torch.randn(*s, dtype=torch.bfloat16) * 0.2)(H * Dh, D)),
        }
    _A_in = _A_setup()

    def _B_setup():
        B, S, D, F, C = 1, 2048, 640, 1536, 1
        return {
            "x": _w((lambda *s: torch.randn(*s, dtype=torch.bfloat16) * 0.05)(B, S, D)),
            "w_r": _w((lambda *s: torch.randn(*s, dtype=torch.bfloat16) * 1.6)(D)),
            "w_gate": _w((lambda *s: torch.randn(*s, dtype=torch.bfloat16) * 0.2)(D, F)),
            "w_up": _w((lambda *s: torch.randn(*s, dtype=torch.bfloat16) * 0.2)(D, F)),
            "w_down": _w((lambda *s: torch.randn(*s, dtype=torch.bfloat16) * 0.2)(F, D)),
        }
    _B_in = _B_setup()

    return {
        "A_x": _A_in["x"],
        "A_wq": _A_in["wq"],
        "A_wk": _A_in["wk"],
        "A_wv": _A_in["wv"],
        "A_wo": _A_in["wo"],
        "B_x": _B_in["x"],
        "B_w_r": _B_in["w_r"],
        "B_w_gate": _B_in["w_gate"],
        "B_w_up": _B_in["w_up"],
        "B_w_down": _B_in["w_down"],
        "gate_w": _w((lambda *s: torch.randn(*s, dtype=torch.bfloat16) * 0.05)(640) * 16.0),
    }

def reference(A_x, A_wq, A_wk, A_wv, A_wo, B_x, B_w_r, B_w_gate, B_w_up, B_w_down, gate_w):
    def _A_ref(x, wq, wk, wv, wo):
        B, S, D = x.shape
        H = 32
        Dh = D // H
        q = (lambda x, *d: x.permute(*d))((x @ wq).reshape(B, S, H, Dh), 0, 2, 1, 3)
        k = x @ wk
        v = x @ wv
        scores = torch.einsum("bhqd,bkd->bhqk", q, k) * (Dh ** -0.5)
        p = _softmax(scores, axis=-1)
        o = torch.einsum("bhqk,bkd->bhqd", p, v)
        return (lambda x, *d: x.permute(*d))(o, 0, 2, 1, 3).reshape(B, S, H * Dh) @ wo

    def _B_ref(x, w_r, w_gate, w_up, w_down):
        B, S, D = x.shape
        C = 1
        r = x @ w_r
        idx = torch.argsort(-r, axis=-1)[:, :C]
        idx_e = (lambda x, n, axis: torch.repeat_interleave(x, n, dim=axis))(idx.reshape(B, C, 1), D, axis=2)
        x_sel = (lambda x, i, axis: torch.take_along_dim(x, i, dim=axis))(x, idx_e, axis=1)
        g = _silu(x_sel @ w_gate)
        u = x_sel @ w_up
        y = (g * u) @ w_down
        r_sel = _sigmoid((lambda x, i, axis: torch.take_along_dim(x, i, dim=axis))(r, idx, axis=1))
        y = y * r_sel.reshape(B, C, 1)
        b_off = (torch.arange(B) * S).reshape(B, 1)
        flat_idx = (idx + b_off).reshape(B * C)
        zero = (x * 0.0).reshape(B * S, D)
        upd = _scatter_add(zero, flat_idx, y.reshape(B * C, D), axis=0)
        return x + upd.reshape(B, S, D)

    x = A_x
    g = _sigmoid((x * gate_w).sum(-1, keepdims=True))
    a = _A_ref(x, A_wq, A_wk, A_wv, A_wo)
    b = _B_ref(x, B_w_r, B_w_gate, B_w_up, B_w_down)
    return g * a + (1 - g) * b
```

```cpp
#include <hip/hip_runtime.h>
#include <stdint.h>

typedef float f32x2 __attribute__((ext_vector_type(2)));
typedef float f32x4 __attribute__((ext_vector_type(4)));
typedef float f32x8 __attribute__((ext_vector_type(8)));
typedef float f32x16 __attribute__((ext_vector_type(16)));
typedef float f32x32 __attribute__((ext_vector_type(32)));
typedef __bf16 bf16x2 __attribute__((ext_vector_type(2)));
typedef __bf16 bf16x8 __attribute__((ext_vector_type(8)));
typedef __bf16 bf16x16 __attribute__((ext_vector_type(16)));
typedef short s16x4 __attribute__((ext_vector_type(4)));
typedef unsigned u32x2 __attribute__((ext_vector_type(2)));
typedef unsigned u32x4 __attribute__((ext_vector_type(4)));
typedef unsigned u32x8 __attribute__((ext_vector_type(8)));
typedef unsigned long long u64;
#define LAS __attribute__((address_space(3)))
typedef LAS char lchar;
typedef __amdgpu_buffer_rsrc_t rsrc_t;

constexpr unsigned WS_EPOCH = 0, WS_G = 49152, WS_KV = 65536, WS_FLAG = 229376, WS_FLAG2 = 262144,
                   WS_H = 294912,
                   WS_Y = WS_H + 245760,
                   WS_O = WS_Y + 122880, WS_END = WS_O + 2048 * 640 * 2;
constexpr unsigned L_W = 0, L_RING = 102400, L_Q = L_RING + 49152, L_MISC = L_Q + 10240, L_END = L_MISC + 1024;
constexpr unsigned L_WKV = L_RING, L_RG = L_RING + 25600, L_XK = L_RG + 2560;
enum { M_TAG = 0, M_SEL, M_SOK, M_KVIN, M_KVOUT, M_KEY, M_TOK, M_XISS, M_XT, M_WQ, M_ISS, M_RDYB, M_QDONE, M_FR, M_AGO, M_WO, M_F1,
       M_YOK, M_SN, M_T0, M_EPA, M_EPB, M_RING = 24, M_XIN = 40, M_FIN = 48, M_TPB, M_TPA, M_GSEL, M_RED = 64 };
static_assert(M_FR == M_QDONE + 1 && M_AGO == M_QDONE + 2 && M_WO == M_QDONE + 3, "asm offsets");
constexpr unsigned SPIN_CAP = 1u << 22;
constexpr int SC1 = 16;
#define B_START 4u
#define PACE 20

#define WAITV(n) asm volatile("s_waitcnt vmcnt(" #n ")" ::: "memory")
#define WAITL0() asm volatile("s_waitcnt lgkmcnt(0)" ::: "memory")
#define BAR() do { WAITL0(); __builtin_amdgcn_s_barrier(); asm volatile("" ::: "memory"); } while (0)


#define ASM_VINIT \
    "v_mov_b32 v184, %[c]\nv_mov_b32 v185, %[c]\nv_mov_b32 v186, %[c]\nv_mov_b32 v187, %[c]\nv_mov_b32 v188, %[c]\nv_mov_b32 v189, %[c]\n" \
    "v_mov_b32 v190, %[c]\nv_mov_b32 v191, %[c]\nv_mov_b32 v192, %[c]\nv_mov_b32 v193, %[c]\nv_mov_b32 v194, %[c]\nv_mov_b32 v195, %[c]\n" \
    "v_mov_b32 v196, %[c]\nv_mov_b32 v197, %[c]\nv_mov_b32 v198, %[c]\nv_mov_b32 v199, %[c]\nv_mov_b32 v200, %[c]\nv_mov_b32 v201, %[c]\n" \
    "v_mov_b32 v202, %[c]\nv_mov_b32 v203, %[c]\nv_mov_b32 v204, %[c]\nv_mov_b32 v205, %[c]\nv_mov_b32 v206, %[c]\nv_mov_b32 v207, %[c]\n" \
    "v_mov_b32 v208, %[c]\nv_mov_b32 v209, %[c]\nv_mov_b32 v210, %[c]\nv_mov_b32 v211, %[c]\nv_mov_b32 v212, %[c]\nv_mov_b32 v213, %[c]\n" \
    "v_mov_b32 v214, %[c]\nv_mov_b32 v215, %[c]\nv_mov_b32 v216, %[c]\nv_mov_b32 v217, %[c]\nv_mov_b32 v218, %[c]\nv_mov_b32 v219, %[c]\n" \
    "v_mov_b32 v220, %[c]\nv_mov_b32 v221, %[c]\nv_mov_b32 v222, %[c]\nv_mov_b32 v223, %[c]\nv_mov_b32 v224, %[c]\nv_mov_b32 v225, %[c]\n" \
    "v_mov_b32 v226, %[c]\nv_mov_b32 v227, %[c]\nv_mov_b32 v228, %[c]\nv_mov_b32 v229, %[c]\nv_mov_b32 v230, %[c]\nv_mov_b32 v231, %[c]\n" \
    "v_mov_b32 v232, %[c]\nv_mov_b32 v233, %[c]\nv_mov_b32 v234, %[c]\nv_mov_b32 v235, %[c]\nv_mov_b32 v236, %[c]\nv_mov_b32 v237, %[c]\n" \
    "v_mov_b32 v238, %[c]\nv_mov_b32 v239, %[c]\nv_mov_b32 v240, %[c]\nv_mov_b32 v241, %[c]\nv_mov_b32 v242, %[c]\nv_mov_b32 v243, %[c]\n" \
    "v_mov_b32 v244, %[c]\nv_mov_b32 v245, %[c]\nv_mov_b32 v246, %[c]\nv_mov_b32 v247, %[c]\n"
#define ASM_KVLOAD1 \
    "s_add_i32 %[st], %[rot], 0\ns_and_b32 %[st], %[st], 3\ns_mulk_i32 %[st], 5120\ns_add_i32 %[st], %[st], %[sb]\n" \
    "buffer_load_dwordx2 v[248:249], %[k2o], %[rs], %[st] offen sc1\nbuffer_load_dwordx4 v[152:155], %[ko], %[rs], %[st] offen offset:0 sc1\n" \
    "buffer_load_dwordx4 v[156:159], %[ko], %[rs], %[st] offen offset:1024 sc1\ns_add_i32 %[st], %[rot], 1\ns_and_b32 %[st], %[st], 3\n" \
    "s_mulk_i32 %[st], 5120\ns_add_i32 %[st], %[st], %[sb]\nbuffer_load_dwordx2 v[250:251], %[k2o], %[rs], %[st] offen sc1\n" \
    "buffer_load_dwordx4 v[160:163], %[ko], %[rs], %[st] offen offset:0 sc1\nbuffer_load_dwordx4 v[164:167], %[ko], %[rs], %[st] offen offset:1024 sc1\n" \
    "s_add_i32 %[st], %[rot], 0\ns_and_b32 %[st], %[st], 3\ns_mulk_i32 %[st], 5120\ns_add_i32 %[st], %[st], %[sb]\ns_mov_b32 exec_lo, 0xfffff\n" \
    "s_mov_b32 exec_hi, 0xfffff\nbuffer_load_dwordx4 v[184:187], %[vo], %[rs], %[st] offen offset:0 sc1\n" \
    "buffer_load_dwordx4 v[188:191], %[vo], %[rs], %[st] offen offset:640 sc1\nbuffer_load_dwordx4 v[192:195], %[vo], %[rs], %[st] offen offset:1280 sc1\n" \
    "buffer_load_dwordx4 v[196:199], %[vo], %[rs], %[st] offen offset:1920 sc1\ns_add_i32 %[st], %[rot], 2\ns_and_b32 %[st], %[st], 3\n" \
    "s_mulk_i32 %[st], 5120\ns_add_i32 %[st], %[st], %[sb]\ns_mov_b32 exec_lo, -1\ns_mov_b32 exec_hi, -1\n" \
    "buffer_load_dwordx2 v[252:253], %[k2o], %[rs], %[st] offen sc1\nbuffer_load_dwordx4 v[168:171], %[ko], %[rs], %[st] offen offset:0 sc1\n" \
    "buffer_load_dwordx4 v[172:175], %[ko], %[rs], %[st] offen offset:1024 sc1\ns_mov_b64 exec, -1\n"
#define ASM_KVLOAD2 \
    "s_add_i32 %[st], %[rot], 1\ns_and_b32 %[st], %[st], 3\ns_mulk_i32 %[st], 5120\ns_add_i32 %[st], %[st], %[sb]\ns_mov_b32 exec_lo, 0xfffff\n" \
    "s_mov_b32 exec_hi, 0xfffff\nbuffer_load_dwordx4 v[200:203], %[vo], %[rs], %[st] offen offset:0 sc1\n" \
    "buffer_load_dwordx4 v[204:207], %[vo], %[rs], %[st] offen offset:640 sc1\nbuffer_load_dwordx4 v[208:211], %[vo], %[rs], %[st] offen offset:1280 sc1\n" \
    "buffer_load_dwordx4 v[212:215], %[vo], %[rs], %[st] offen offset:1920 sc1\ns_add_i32 %[st], %[rot], 3\ns_and_b32 %[st], %[st], 3\n" \
    "s_mulk_i32 %[st], 5120\ns_add_i32 %[st], %[st], %[sb]\ns_mov_b32 exec_lo, -1\ns_mov_b32 exec_hi, -1\n" \
    "buffer_load_dwordx2 v[254:255], %[k2o], %[rs], %[st] offen sc1\nbuffer_load_dwordx4 v[176:179], %[ko], %[rs], %[st] offen offset:0 sc1\n" \
    "buffer_load_dwordx4 v[180:183], %[ko], %[rs], %[st] offen offset:1024 sc1\ns_add_i32 %[st], %[rot], 2\ns_and_b32 %[st], %[st], 3\n" \
    "s_mulk_i32 %[st], 5120\ns_add_i32 %[st], %[st], %[sb]\ns_mov_b32 exec_lo, 0xfffff\ns_mov_b32 exec_hi, 0xfffff\n" \
    "buffer_load_dwordx4 v[216:219], %[vo], %[rs], %[st] offen offset:0 sc1\nbuffer_load_dwordx4 v[220:223], %[vo], %[rs], %[st] offen offset:640 sc1\n" \
    "buffer_load_dwordx4 v[224:227], %[vo], %[rs], %[st] offen offset:1280 sc1\nbuffer_load_dwordx4 v[228:231], %[vo], %[rs], %[st] offen offset:1920 sc1\n" \
    "s_add_i32 %[st], %[rot], 3\ns_and_b32 %[st], %[st], 3\ns_mulk_i32 %[st], 5120\ns_add_i32 %[st], %[st], %[sb]\n" \
    "buffer_load_dwordx4 v[232:235], %[vo], %[rs], %[st] offen offset:0 sc1\nbuffer_load_dwordx4 v[236:239], %[vo], %[rs], %[st] offen offset:640 sc1\n" \
    "buffer_load_dwordx4 v[240:243], %[vo], %[rs], %[st] offen offset:1280 sc1\nbuffer_load_dwordx4 v[244:247], %[vo], %[rs], %[st] offen offset:1920 sc1\n" \
    "s_mov_b64 exec, -1\n"
#define ASM_GEMM1 \
    "ds_read_b32 v31, %[ma]\ns_waitcnt lgkmcnt(0)\nv_readfirstlane_b32 %[st], v31\ns_bitcmp1_b32 %[st], 0\ns_cbranch_scc1 .Lg0_%=\n.Lw0_%=:\n" \
    "ds_read_b32 v31, %[ma]\ns_waitcnt lgkmcnt(0)\nv_readfirstlane_b32 %[st], v31\ns_add_i32 %[cap], %[cap], -1\ns_cmp_le_i32 %[cap], 0\n" \
    "s_cbranch_scc1 .Lg0_%=\ns_bitcmp1_b32 %[st], 0\ns_cbranch_scc0 .Lw0_%=\n.Lg0_%=:\nds_read_b64_tr_b16 v[152:153], %[wb1] offset:0\n" \
    "ds_read_b64_tr_b16 v[154:155], %[wb2] offset:0\nds_read_b32 v31, %[ma]\nds_read_b64_tr_b16 v[156:157], %[wb1] offset:32\n" \
    "ds_read_b64_tr_b16 v[158:159], %[wb2] offset:32\nds_read_b64_tr_b16 v[160:161], %[wb1] offset:64\nds_read_b64_tr_b16 v[162:163], %[wb2] offset:64\n" \
    "ds_read_b64_tr_b16 v[164:165], %[wb1] offset:96\nds_read_b64_tr_b16 v[166:167], %[wb2] offset:96\nds_read_b64_tr_b16 v[168:169], %[wb1] offset:128\n" \
    "ds_read_b64_tr_b16 v[170:171], %[wb2] offset:128\ns_waitcnt lgkmcnt(9)\nv_mfma_f32_16x16x32_bf16 v[32:35], v[152:155], v[72:75], 0\n" \
    "s_waitcnt lgkmcnt(8)\nv_readfirstlane_b32 %[st], v31\ns_bitcmp1_b32 %[st], 1\ns_cbranch_scc1 .Lg1_%=\n.Lw1_%=:\nds_read_b32 v31, %[ma]\n" \
    "s_waitcnt lgkmcnt(0)\nv_readfirstlane_b32 %[st], v31\ns_add_i32 %[cap], %[cap], -1\ns_cmp_le_i32 %[cap], 0\ns_cbranch_scc1 .Lg1_%=\n" \
    "s_bitcmp1_b32 %[st], 1\ns_cbranch_scc0 .Lw1_%=\n.Lg1_%=:\nds_read_b64_tr_b16 v[172:173], %[wb1] offset:10240\n" \
    "ds_read_b64_tr_b16 v[174:175], %[wb2] offset:10240\nds_read_b32 v31, %[ma]\nv_mfma_f32_16x16x32_bf16 v[52:55], v[152:155], v[76:79], 0\n" \
    "s_waitcnt lgkmcnt(9)\nv_mfma_f32_16x16x32_bf16 v[36:39], v[156:159], v[72:75], 0\nds_read_b64_tr_b16 v[176:177], %[wb1] offset:10272\n" \
    "ds_read_b64_tr_b16 v[178:179], %[wb2] offset:10272\nv_mfma_f32_16x16x32_bf16 v[56:59], v[156:159], v[76:79], 0\ns_waitcnt lgkmcnt(9)\n" \
    "v_mfma_f32_16x16x32_bf16 v[40:43], v[160:163], v[72:75], 0\nds_read_b64_tr_b16 v[180:181], %[wb1] offset:10304\n" \
    "ds_read_b64_tr_b16 v[182:183], %[wb2] offset:10304\nv_mfma_f32_16x16x32_bf16 v[60:63], v[160:163], v[76:79], 0\ns_waitcnt lgkmcnt(9)\n" \
    "v_mfma_f32_16x16x32_bf16 v[44:47], v[164:167], v[72:75], 0\nds_read_b64_tr_b16 v[152:153], %[wb1] offset:10336\n" \
    "ds_read_b64_tr_b16 v[154:155], %[wb2] offset:10336\nv_mfma_f32_16x16x32_bf16 v[64:67], v[164:167], v[76:79], 0\ns_waitcnt lgkmcnt(9)\n" \
    "v_mfma_f32_16x16x32_bf16 v[48:51], v[168:171], v[72:75], 0\nds_read_b64_tr_b16 v[156:157], %[wb1] offset:10368\n" \
    "ds_read_b64_tr_b16 v[158:159], %[wb2] offset:10368\nv_mfma_f32_16x16x32_bf16 v[68:71], v[168:171], v[76:79], 0\ns_waitcnt lgkmcnt(9)\n" \
    "v_mfma_f32_16x16x32_bf16 v[32:35], v[172:175], v[80:83], v[32:35]\ns_waitcnt lgkmcnt(8)\nv_readfirstlane_b32 %[st], v31\ns_bitcmp1_b32 %[st], 2\n" \
    "s_cbranch_scc1 .Lg2_%=\n.Lw2_%=:\nds_read_b32 v31, %[ma]\ns_waitcnt lgkmcnt(0)\nv_readfirstlane_b32 %[st], v31\ns_add_i32 %[cap], %[cap], -1\n" \
    "s_cmp_le_i32 %[cap], 0\ns_cbranch_scc1 .Lg2_%=\ns_bitcmp1_b32 %[st], 2\ns_cbranch_scc0 .Lw2_%=\n.Lg2_%=:\n" \
    "ds_read_b64_tr_b16 v[160:161], %[wb1] offset:20480\nds_read_b64_tr_b16 v[162:163], %[wb2] offset:20480\nds_read_b32 v31, %[ma]\n" \
    "v_mfma_f32_16x16x32_bf16 v[52:55], v[172:175], v[84:87], v[52:55]\ns_waitcnt lgkmcnt(9)\n" \
    "v_mfma_f32_16x16x32_bf16 v[36:39], v[176:179], v[80:83], v[36:39]\nds_read_b64_tr_b16 v[164:165], %[wb1] offset:20512\n" \
    "ds_read_b64_tr_b16 v[166:167], %[wb2] offset:20512\nv_mfma_f32_16x16x32_bf16 v[56:59], v[176:179], v[84:87], v[56:59]\ns_waitcnt lgkmcnt(9)\n" \
    "v_mfma_f32_16x16x32_bf16 v[40:43], v[180:183], v[80:83], v[40:43]\nds_read_b64_tr_b16 v[168:169], %[wb1] offset:20544\n" \
    "ds_read_b64_tr_b16 v[170:171], %[wb2] offset:20544\nv_mfma_f32_16x16x32_bf16 v[60:63], v[180:183], v[84:87], v[60:63]\ns_waitcnt lgkmcnt(9)\n" \
    "v_mfma_f32_16x16x32_bf16 v[44:47], v[152:155], v[80:83], v[44:47]\nds_read_b64_tr_b16 v[172:173], %[wb1] offset:20576\n" \
    "ds_read_b64_tr_b16 v[174:175], %[wb2] offset:20576\nv_mfma_f32_16x16x32_bf16 v[64:67], v[152:155], v[84:87], v[64:67]\ns_waitcnt lgkmcnt(9)\n" \
    "v_mfma_f32_16x16x32_bf16 v[48:51], v[156:159], v[80:83], v[48:51]\nds_read_b64_tr_b16 v[176:177], %[wb1] offset:20608\n" \
    "ds_read_b64_tr_b16 v[178:179], %[wb2] offset:20608\nv_mfma_f32_16x16x32_bf16 v[68:71], v[156:159], v[84:87], v[68:71]\ns_waitcnt lgkmcnt(9)\n" \
    "v_mfma_f32_16x16x32_bf16 v[32:35], v[160:163], v[88:91], v[32:35]\ns_waitcnt lgkmcnt(8)\nv_readfirstlane_b32 %[st], v31\ns_bitcmp1_b32 %[st], 3\n" \
    "s_cbranch_scc1 .Lg3_%=\n.Lw3_%=:\nds_read_b32 v31, %[ma]\ns_waitcnt lgkmcnt(0)\nv_readfirstlane_b32 %[st], v31\ns_add_i32 %[cap], %[cap], -1\n" \
    "s_cmp_le_i32 %[cap], 0\ns_cbranch_scc1 .Lg3_%=\ns_bitcmp1_b32 %[st], 3\ns_cbranch_scc0 .Lw3_%=\n.Lg3_%=:\n" \
    "ds_read_b64_tr_b16 v[180:181], %[wb1] offset:30720\nds_read_b64_tr_b16 v[182:183], %[wb2] offset:30720\nds_read_b32 v31, %[ma]\n" \
    "v_mfma_f32_16x16x32_bf16 v[52:55], v[160:163], v[92:95], v[52:55]\ns_waitcnt lgkmcnt(9)\n" \
    "v_mfma_f32_16x16x32_bf16 v[36:39], v[164:167], v[88:91], v[36:39]\nds_read_b64_tr_b16 v[152:153], %[wb1] offset:30752\n" \
    "ds_read_b64_tr_b16 v[154:155], %[wb2] offset:30752\nv_mfma_f32_16x16x32_bf16 v[56:59], v[164:167], v[92:95], v[56:59]\ns_waitcnt lgkmcnt(9)\n" \
    "v_mfma_f32_16x16x32_bf16 v[40:43], v[168:171], v[88:91], v[40:43]\nds_read_b64_tr_b16 v[156:157], %[wb1] offset:30784\n" \
    "ds_read_b64_tr_b16 v[158:159], %[wb2] offset:30784\nv_mfma_f32_16x16x32_bf16 v[60:63], v[168:171], v[92:95], v[60:63]\ns_waitcnt lgkmcnt(9)\n" \
    "v_mfma_f32_16x16x32_bf16 v[44:47], v[172:175], v[88:91], v[44:47]\nds_read_b64_tr_b16 v[160:161], %[wb1] offset:30816\n" \
    "ds_read_b64_tr_b16 v[162:163], %[wb2] offset:30816\nv_mfma_f32_16x16x32_bf16 v[64:67], v[172:175], v[92:95], v[64:67]\ns_waitcnt lgkmcnt(9)\n" \
    "v_mfma_f32_16x16x32_bf16 v[48:51], v[176:179], v[88:91], v[48:51]\nds_read_b64_tr_b16 v[164:165], %[wb1] offset:30848\n" \
    "ds_read_b64_tr_b16 v[166:167], %[wb2] offset:30848\nv_mfma_f32_16x16x32_bf16 v[68:71], v[176:179], v[92:95], v[68:71]\ns_waitcnt lgkmcnt(9)\n" \
    "v_mfma_f32_16x16x32_bf16 v[32:35], v[180:183], v[96:99], v[32:35]\ns_waitcnt lgkmcnt(8)\nv_readfirstlane_b32 %[st], v31\ns_bitcmp1_b32 %[st], 4\n" \
    "s_cbranch_scc1 .Lg4_%=\n.Lw4_%=:\nds_read_b32 v31, %[ma]\ns_waitcnt lgkmcnt(0)\nv_readfirstlane_b32 %[st], v31\ns_add_i32 %[cap], %[cap], -1\n" \
    "s_cmp_le_i32 %[cap], 0\ns_cbranch_scc1 .Lg4_%=\ns_bitcmp1_b32 %[st], 4\ns_cbranch_scc0 .Lw4_%=\n.Lg4_%=:\n" \
    "ds_read_b64_tr_b16 v[168:169], %[wb1] offset:40960\nds_read_b64_tr_b16 v[170:171], %[wb2] offset:40960\nds_read_b32 v31, %[ma]\n" \
    "v_mfma_f32_16x16x32_bf16 v[52:55], v[180:183], v[100:103], v[52:55]\ns_waitcnt lgkmcnt(9)\n" \
    "v_mfma_f32_16x16x32_bf16 v[36:39], v[152:155], v[96:99], v[36:39]\nds_read_b64_tr_b16 v[172:173], %[wb1] offset:40992\n" \
    "ds_read_b64_tr_b16 v[174:175], %[wb2] offset:40992\nv_mfma_f32_16x16x32_bf16 v[56:59], v[152:155], v[100:103], v[56:59]\ns_waitcnt lgkmcnt(9)\n" \
    "v_mfma_f32_16x16x32_bf16 v[40:43], v[156:159], v[96:99], v[40:43]\nds_read_b64_tr_b16 v[176:177], %[wb1] offset:41024\n" \
    "ds_read_b64_tr_b16 v[178:179], %[wb2] offset:41024\nv_mfma_f32_16x16x32_bf16 v[60:63], v[156:159], v[100:103], v[60:63]\ns_waitcnt lgkmcnt(9)\n" \
    "v_mfma_f32_16x16x32_bf16 v[44:47], v[160:163], v[96:99], v[44:47]\nds_read_b64_tr_b16 v[180:181], %[wb1] offset:41056\n" \
    "ds_read_b64_tr_b16 v[182:183], %[wb2] offset:41056\nv_mfma_f32_16x16x32_bf16 v[64:67], v[160:163], v[100:103], v[64:67]\ns_waitcnt lgkmcnt(9)\n" \
    "v_mfma_f32_16x16x32_bf16 v[48:51], v[164:167], v[96:99], v[48:51]\nds_read_b64_tr_b16 v[152:153], %[wb1] offset:41088\n" \
    "ds_read_b64_tr_b16 v[154:155], %[wb2] offset:41088\nv_mfma_f32_16x16x32_bf16 v[68:71], v[164:167], v[100:103], v[68:71]\ns_waitcnt lgkmcnt(9)\n" \
    "v_mfma_f32_16x16x32_bf16 v[32:35], v[168:171], v[104:107], v[32:35]\ns_waitcnt lgkmcnt(8)\nv_readfirstlane_b32 %[st], v31\ns_bitcmp1_b32 %[st], 5\n" \
    "s_cbranch_scc1 .Lg5_%=\n.Lw5_%=:\nds_read_b32 v31, %[ma]\ns_waitcnt lgkmcnt(0)\nv_readfirstlane_b32 %[st], v31\ns_add_i32 %[cap], %[cap], -1\n" \
    "s_cmp_le_i32 %[cap], 0\ns_cbranch_scc1 .Lg5_%=\ns_bitcmp1_b32 %[st], 5\ns_cbranch_scc0 .Lw5_%=\n.Lg5_%=:\nv_add_u32 %[wb1], 51200, %[wb1]\n" \
    "v_add_u32 %[wb2], 51200, %[wb2]\nds_read_b64_tr_b16 v[156:157], %[wb1] offset:0\nds_read_b64_tr_b16 v[158:159], %[wb2] offset:0\n" \
    "ds_read_b32 v31, %[ma]\nv_mfma_f32_16x16x32_bf16 v[52:55], v[168:171], v[108:111], v[52:55]\ns_waitcnt lgkmcnt(9)\n" \
    "v_mfma_f32_16x16x32_bf16 v[36:39], v[172:175], v[104:107], v[36:39]\nds_read_b64_tr_b16 v[160:161], %[wb1] offset:32\n" \
    "ds_read_b64_tr_b16 v[162:163], %[wb2] offset:32\nv_mfma_f32_16x16x32_bf16 v[56:59], v[172:175], v[108:111], v[56:59]\ns_waitcnt lgkmcnt(9)\n" \
    "v_mfma_f32_16x16x32_bf16 v[40:43], v[176:179], v[104:107], v[40:43]\nds_read_b64_tr_b16 v[164:165], %[wb1] offset:64\n" \
    "ds_read_b64_tr_b16 v[166:167], %[wb2] offset:64\nv_mfma_f32_16x16x32_bf16 v[60:63], v[176:179], v[108:111], v[60:63]\ns_waitcnt lgkmcnt(9)\n" \
    "v_mfma_f32_16x16x32_bf16 v[44:47], v[180:183], v[104:107], v[44:47]\nds_read_b64_tr_b16 v[168:169], %[wb1] offset:96\n" \
    "ds_read_b64_tr_b16 v[170:171], %[wb2] offset:96\nv_mfma_f32_16x16x32_bf16 v[64:67], v[180:183], v[108:111], v[64:67]\ns_waitcnt lgkmcnt(9)\n" \
    "v_mfma_f32_16x16x32_bf16 v[48:51], v[152:155], v[104:107], v[48:51]\nds_read_b64_tr_b16 v[172:173], %[wb1] offset:128\n" \
    "ds_read_b64_tr_b16 v[174:175], %[wb2] offset:128\nv_mfma_f32_16x16x32_bf16 v[68:71], v[152:155], v[108:111], v[68:71]\ns_waitcnt lgkmcnt(9)\n" \
    "v_mfma_f32_16x16x32_bf16 v[32:35], v[156:159], v[112:115], v[32:35]\ns_waitcnt lgkmcnt(8)\nv_readfirstlane_b32 %[st], v31\ns_bitcmp1_b32 %[st], 6\n" \
    "s_cbranch_scc1 .Lg6_%=\n.Lw6_%=:\nds_read_b32 v31, %[ma]\ns_waitcnt lgkmcnt(0)\nv_readfirstlane_b32 %[st], v31\ns_add_i32 %[cap], %[cap], -1\n" \
    "s_cmp_le_i32 %[cap], 0\ns_cbranch_scc1 .Lg6_%=\ns_bitcmp1_b32 %[st], 6\ns_cbranch_scc0 .Lw6_%=\n.Lg6_%=:\n" \
    "ds_read_b64_tr_b16 v[176:177], %[wb1] offset:10240\nds_read_b64_tr_b16 v[178:179], %[wb2] offset:10240\nds_read_b32 v31, %[ma]\n" \
    "v_mfma_f32_16x16x32_bf16 v[52:55], v[156:159], v[116:119], v[52:55]\ns_waitcnt lgkmcnt(9)\n" \
    "v_mfma_f32_16x16x32_bf16 v[36:39], v[160:163], v[112:115], v[36:39]\nds_read_b64_tr_b16 v[180:181], %[wb1] offset:10272\n" \
    "ds_read_b64_tr_b16 v[182:183], %[wb2] offset:10272\nv_mfma_f32_16x16x32_bf16 v[56:59], v[160:163], v[116:119], v[56:59]\ns_waitcnt lgkmcnt(9)\n" \
    "v_mfma_f32_16x16x32_bf16 v[40:43], v[164:167], v[112:115], v[40:43]\nds_read_b64_tr_b16 v[152:153], %[wb1] offset:10304\n" \
    "ds_read_b64_tr_b16 v[154:155], %[wb2] offset:10304\nv_mfma_f32_16x16x32_bf16 v[60:63], v[164:167], v[116:119], v[60:63]\ns_waitcnt lgkmcnt(9)\n" \
    "v_mfma_f32_16x16x32_bf16 v[44:47], v[168:171], v[112:115], v[44:47]\nds_read_b64_tr_b16 v[156:157], %[wb1] offset:10336\n" \
    "ds_read_b64_tr_b16 v[158:159], %[wb2] offset:10336\nv_mfma_f32_16x16x32_bf16 v[64:67], v[168:171], v[116:119], v[64:67]\ns_waitcnt lgkmcnt(9)\n" \
    "v_mfma_f32_16x16x32_bf16 v[48:51], v[172:175], v[112:115], v[48:51]\nds_read_b64_tr_b16 v[160:161], %[wb1] offset:10368\n" \
    "ds_read_b64_tr_b16 v[162:163], %[wb2] offset:10368\nv_mfma_f32_16x16x32_bf16 v[68:71], v[172:175], v[116:119], v[68:71]\ns_waitcnt lgkmcnt(9)\n" \
    "v_mfma_f32_16x16x32_bf16 v[32:35], v[176:179], v[120:123], v[32:35]\ns_waitcnt lgkmcnt(8)\nv_readfirstlane_b32 %[st], v31\ns_bitcmp1_b32 %[st], 7\n" \
    "s_cbranch_scc1 .Lg7_%=\n.Lw7_%=:\nds_read_b32 v31, %[ma]\ns_waitcnt lgkmcnt(0)\nv_readfirstlane_b32 %[st], v31\ns_add_i32 %[cap], %[cap], -1\n" \
    "s_cmp_le_i32 %[cap], 0\ns_cbranch_scc1 .Lg7_%=\ns_bitcmp1_b32 %[st], 7\ns_cbranch_scc0 .Lw7_%=\n.Lg7_%=:\n" \
    "ds_read_b64_tr_b16 v[164:165], %[wb1] offset:20480\nds_read_b64_tr_b16 v[166:167], %[wb2] offset:20480\nds_read_b32 v31, %[ma]\n" \
    "v_mfma_f32_16x16x32_bf16 v[52:55], v[176:179], v[124:127], v[52:55]\ns_waitcnt lgkmcnt(9)\n" \
    "v_mfma_f32_16x16x32_bf16 v[36:39], v[180:183], v[120:123], v[36:39]\nds_read_b64_tr_b16 v[168:169], %[wb1] offset:20512\n" \
    "ds_read_b64_tr_b16 v[170:171], %[wb2] offset:20512\nv_mfma_f32_16x16x32_bf16 v[56:59], v[180:183], v[124:127], v[56:59]\ns_waitcnt lgkmcnt(9)\n" \
    "v_mfma_f32_16x16x32_bf16 v[40:43], v[152:155], v[120:123], v[40:43]\nds_read_b64_tr_b16 v[172:173], %[wb1] offset:20544\n" \
    "ds_read_b64_tr_b16 v[174:175], %[wb2] offset:20544\nv_mfma_f32_16x16x32_bf16 v[60:63], v[152:155], v[124:127], v[60:63]\ns_waitcnt lgkmcnt(9)\n" \
    "v_mfma_f32_16x16x32_bf16 v[44:47], v[156:159], v[120:123], v[44:47]\nds_read_b64_tr_b16 v[176:177], %[wb1] offset:20576\n" \
    "ds_read_b64_tr_b16 v[178:179], %[wb2] offset:20576\nv_mfma_f32_16x16x32_bf16 v[64:67], v[156:159], v[124:127], v[64:67]\ns_waitcnt lgkmcnt(9)\n" \
    "v_mfma_f32_16x16x32_bf16 v[48:51], v[160:163], v[120:123], v[48:51]\nds_read_b64_tr_b16 v[180:181], %[wb1] offset:20608\n" \
    "ds_read_b64_tr_b16 v[182:183], %[wb2] offset:20608\nv_mfma_f32_16x16x32_bf16 v[68:71], v[160:163], v[124:127], v[68:71]\ns_waitcnt lgkmcnt(9)\n" \
    "v_mfma_f32_16x16x32_bf16 v[32:35], v[164:167], v[128:131], v[32:35]\ns_waitcnt lgkmcnt(8)\nv_readfirstlane_b32 %[st], v31\ns_bitcmp1_b32 %[st], 8\n" \
    "s_cbranch_scc1 .Lg8_%=\n.Lw8_%=:\nds_read_b32 v31, %[ma]\ns_waitcnt lgkmcnt(0)\nv_readfirstlane_b32 %[st], v31\ns_add_i32 %[cap], %[cap], -1\n" \
    "s_cmp_le_i32 %[cap], 0\ns_cbranch_scc1 .Lg8_%=\ns_bitcmp1_b32 %[st], 8\ns_cbranch_scc0 .Lw8_%=\n.Lg8_%=:\n" \
    "ds_read_b64_tr_b16 v[152:153], %[wb1] offset:30720\nds_read_b64_tr_b16 v[154:155], %[wb2] offset:30720\nds_read_b32 v31, %[ma]\n" \
    "v_mfma_f32_16x16x32_bf16 v[52:55], v[164:167], v[132:135], v[52:55]\ns_waitcnt lgkmcnt(9)\n" \
    "v_mfma_f32_16x16x32_bf16 v[36:39], v[168:171], v[128:131], v[36:39]\nds_read_b64_tr_b16 v[156:157], %[wb1] offset:30752\n" \
    "ds_read_b64_tr_b16 v[158:159], %[wb2] offset:30752\nv_mfma_f32_16x16x32_bf16 v[56:59], v[168:171], v[132:135], v[56:59]\ns_waitcnt lgkmcnt(9)\n" \
    "v_mfma_f32_16x16x32_bf16 v[40:43], v[172:175], v[128:131], v[40:43]\nds_read_b64_tr_b16 v[160:161], %[wb1] offset:30784\n" \
    "ds_read_b64_tr_b16 v[162:163], %[wb2] offset:30784\nv_mfma_f32_16x16x32_bf16 v[60:63], v[172:175], v[132:135], v[60:63]\ns_waitcnt lgkmcnt(9)\n" \
    "v_mfma_f32_16x16x32_bf16 v[44:47], v[176:179], v[128:131], v[44:47]\nds_read_b64_tr_b16 v[164:165], %[wb1] offset:30816\n" \
    "ds_read_b64_tr_b16 v[166:167], %[wb2] offset:30816\nv_mfma_f32_16x16x32_bf16 v[64:67], v[176:179], v[132:135], v[64:67]\ns_waitcnt lgkmcnt(9)\n" \
    "v_mfma_f32_16x16x32_bf16 v[48:51], v[180:183], v[128:131], v[48:51]\nds_read_b64_tr_b16 v[168:169], %[wb1] offset:30848\n" \
    "ds_read_b64_tr_b16 v[170:171], %[wb2] offset:30848\nv_mfma_f32_16x16x32_bf16 v[68:71], v[180:183], v[132:135], v[68:71]\ns_waitcnt lgkmcnt(9)\n" \
    "v_mfma_f32_16x16x32_bf16 v[32:35], v[152:155], v[136:139], v[32:35]\ns_waitcnt lgkmcnt(8)\nv_readfirstlane_b32 %[st], v31\ns_bitcmp1_b32 %[st], 9\n" \
    "s_cbranch_scc1 .Lg9_%=\n.Lw9_%=:\nds_read_b32 v31, %[ma]\ns_waitcnt lgkmcnt(0)\nv_readfirstlane_b32 %[st], v31\ns_add_i32 %[cap], %[cap], -1\n" \
    "s_cmp_le_i32 %[cap], 0\ns_cbranch_scc1 .Lg9_%=\ns_bitcmp1_b32 %[st], 9\ns_cbranch_scc0 .Lw9_%=\n.Lg9_%=:\n" \
    "ds_read_b64_tr_b16 v[172:173], %[wb1] offset:40960\nds_read_b64_tr_b16 v[174:175], %[wb2] offset:40960\n" \
    "v_mfma_f32_16x16x32_bf16 v[52:55], v[152:155], v[140:143], v[52:55]\ns_waitcnt lgkmcnt(8)\n" \
    "v_mfma_f32_16x16x32_bf16 v[36:39], v[156:159], v[136:139], v[36:39]\nds_read_b64_tr_b16 v[176:177], %[wb1] offset:40992\n" \
    "ds_read_b64_tr_b16 v[178:179], %[wb2] offset:40992\nv_mfma_f32_16x16x32_bf16 v[56:59], v[156:159], v[140:143], v[56:59]\ns_waitcnt lgkmcnt(8)\n" \
    "v_mfma_f32_16x16x32_bf16 v[40:43], v[160:163], v[136:139], v[40:43]\nds_read_b64_tr_b16 v[180:181], %[wb1] offset:41024\n" \
    "ds_read_b64_tr_b16 v[182:183], %[wb2] offset:41024\nv_mfma_f32_16x16x32_bf16 v[60:63], v[160:163], v[140:143], v[60:63]\ns_waitcnt lgkmcnt(8)\n" \
    "v_mfma_f32_16x16x32_bf16 v[44:47], v[164:167], v[136:139], v[44:47]\nds_read_b64_tr_b16 v[152:153], %[wb1] offset:41056\n" \
    "ds_read_b64_tr_b16 v[154:155], %[wb2] offset:41056\nv_mfma_f32_16x16x32_bf16 v[64:67], v[164:167], v[140:143], v[64:67]\ns_waitcnt lgkmcnt(8)\n" \
    "v_mfma_f32_16x16x32_bf16 v[48:51], v[168:171], v[136:139], v[48:51]\nds_read_b64_tr_b16 v[156:157], %[wb1] offset:41088\n" \
    "ds_read_b64_tr_b16 v[158:159], %[wb2] offset:41088\nv_mfma_f32_16x16x32_bf16 v[68:71], v[168:171], v[140:143], v[68:71]\ns_waitcnt lgkmcnt(8)\n" \
    "v_mfma_f32_16x16x32_bf16 v[32:35], v[172:175], v[144:147], v[32:35]\nv_mfma_f32_16x16x32_bf16 v[52:55], v[172:175], v[148:151], v[52:55]\n" \
    "s_waitcnt lgkmcnt(6)\nv_mfma_f32_16x16x32_bf16 v[36:39], v[176:179], v[144:147], v[36:39]\n" \
    "v_mfma_f32_16x16x32_bf16 v[56:59], v[176:179], v[148:151], v[56:59]\ns_waitcnt lgkmcnt(4)\n" \
    "v_mfma_f32_16x16x32_bf16 v[40:43], v[180:183], v[144:147], v[40:43]\nv_mfma_f32_16x16x32_bf16 v[60:63], v[180:183], v[148:151], v[60:63]\n" \
    "ds_read_b32 v31, %[ma]\ns_waitcnt lgkmcnt(3)\nv_mfma_f32_16x16x32_bf16 v[44:47], v[152:155], v[144:147], v[44:47]\n" \
    "v_mfma_f32_16x16x32_bf16 v[64:67], v[152:155], v[148:151], v[64:67]\ns_waitcnt lgkmcnt(1)\n" \
    "v_mfma_f32_16x16x32_bf16 v[48:51], v[156:159], v[144:147], v[48:51]\nv_mfma_f32_16x16x32_bf16 v[68:71], v[156:159], v[148:151], v[68:71]\n" \
    "s_waitcnt vmcnt(0) lgkmcnt(0)\nv_readfirstlane_b32 %[st], v31\nv_mov_b32 v31, 1\ns_bitcmp1_b32 %[st], 11\ns_cbranch_scc1 .Lkv_%=\n" \
    "ds_write_b128 %[ex], v[32:35] offset:0\nds_write_b128 %[ex], v[52:55] offset:5120\nds_write_b128 %[ex], v[36:39] offset:1024\n" \
    "ds_write_b128 %[ex], v[56:59] offset:6144\nds_write_b128 %[ex], v[40:43] offset:2048\nds_write_b128 %[ex], v[60:63] offset:7168\n" \
    "ds_write_b128 %[ex], v[44:47] offset:3072\nds_write_b128 %[ex], v[64:67] offset:8192\nds_write_b128 %[ex], v[48:51] offset:4096\n" \
    "ds_write_b128 %[ex], v[68:71] offset:9216\ns_mov_b64 exec, 1\nds_add_u32 %[xs], v31\ns_mov_b64 exec, -1\n.Lr_%=:\nds_read_b32 v31, %[ma]\n" \
    "s_waitcnt lgkmcnt(0)\nv_readfirstlane_b32 %[st], v31\ns_add_i32 %[cap], %[cap], -1\ns_cmp_le_i32 %[cap], 0\ns_cbranch_scc1 .Lr2_%=\n" \
    "s_bitcmp1_b32 %[st], 11\ns_cbranch_scc0 .Lr_%=\n.Lr2_%=:\ns_add_i32 %[st], %[rot], 0\ns_and_b32 %[st], %[st], 3\ns_mulk_i32 %[st], 5120\n" \
    "s_add_i32 %[st], %[st], %[sb]\nbuffer_load_dwordx2 v[248:249], %[k2o], %[rs], %[st] offen sc1\n" \
    "buffer_load_dwordx4 v[152:155], %[ko], %[rs], %[st] offen offset:0 sc1\nbuffer_load_dwordx4 v[156:159], %[ko], %[rs], %[st] offen offset:1024 sc1\n" \
    "s_add_i32 %[st], %[rot], 1\ns_and_b32 %[st], %[st], 3\ns_mulk_i32 %[st], 5120\ns_add_i32 %[st], %[st], %[sb]\n" \
    "buffer_load_dwordx2 v[250:251], %[k2o], %[rs], %[st] offen sc1\nbuffer_load_dwordx4 v[160:163], %[ko], %[rs], %[st] offen offset:0 sc1\n" \
    "buffer_load_dwordx4 v[164:167], %[ko], %[rs], %[st] offen offset:1024 sc1\ns_mov_b64 exec, -1\ns_branch .Lend_%=\n.Lkv_%=:\n" \
    "s_add_i32 %[st], %[rot], 0\ns_and_b32 %[st], %[st], 3\ns_mulk_i32 %[st], 5120\ns_add_i32 %[st], %[st], %[sb]\n" \
    "buffer_load_dwordx2 v[248:249], %[k2o], %[rs], %[st] offen sc1\nds_write_b128 %[ex], v[32:35] offset:0\n" \
    "buffer_load_dwordx4 v[152:155], %[ko], %[rs], %[st] offen offset:0 sc1\nds_write_b128 %[ex], v[52:55] offset:5120\n" \
    "buffer_load_dwordx4 v[156:159], %[ko], %[rs], %[st] offen offset:1024 sc1\nds_write_b128 %[ex], v[36:39] offset:1024\ns_add_i32 %[st], %[rot], 1\n" \
    "s_and_b32 %[st], %[st], 3\ns_mulk_i32 %[st], 5120\ns_add_i32 %[st], %[st], %[sb]\nbuffer_load_dwordx2 v[250:251], %[k2o], %[rs], %[st] offen sc1\n" \
    "ds_write_b128 %[ex], v[56:59] offset:6144\nbuffer_load_dwordx4 v[160:163], %[ko], %[rs], %[st] offen offset:0 sc1\n" \
    "ds_write_b128 %[ex], v[40:43] offset:2048\nbuffer_load_dwordx4 v[164:167], %[ko], %[rs], %[st] offen offset:1024 sc1\n" \
    "ds_write_b128 %[ex], v[60:63] offset:7168\nds_write_b128 %[ex], v[44:47] offset:3072\nds_write_b128 %[ex], v[64:67] offset:8192\n" \
    "ds_write_b128 %[ex], v[48:51] offset:4096\nds_write_b128 %[ex], v[68:71] offset:9216\ns_mov_b64 exec, 1\nds_add_u32 %[xs], v31\ns_mov_b64 exec, -1\n" \
    ".Lend_%=:\ns_add_i32 %[st], %[rot], 0\ns_and_b32 %[st], %[st], 3\ns_mulk_i32 %[st], 5120\ns_add_i32 %[st], %[st], %[sb]\ns_mov_b32 exec_lo, 0xfffff\n" \
    "s_mov_b32 exec_hi, 0xfffff\nbuffer_load_dwordx4 v[184:187], %[vo], %[rs], %[st] offen offset:0 sc1\n" \
    "buffer_load_dwordx4 v[188:191], %[vo], %[rs], %[st] offen offset:640 sc1\nbuffer_load_dwordx4 v[192:195], %[vo], %[rs], %[st] offen offset:1280 sc1\n" \
    "buffer_load_dwordx4 v[196:199], %[vo], %[rs], %[st] offen offset:1920 sc1\ns_add_i32 %[st], %[rot], 2\ns_and_b32 %[st], %[st], 3\n" \
    "s_mulk_i32 %[st], 5120\ns_add_i32 %[st], %[st], %[sb]\ns_mov_b32 exec_lo, -1\ns_mov_b32 exec_hi, -1\n" \
    "buffer_load_dwordx2 v[252:253], %[k2o], %[rs], %[st] offen sc1\nbuffer_load_dwordx4 v[168:171], %[ko], %[rs], %[st] offen offset:0 sc1\n" \
    "buffer_load_dwordx4 v[172:175], %[ko], %[rs], %[st] offen offset:1024 sc1\ns_add_i32 %[st], %[rot], 1\ns_and_b32 %[st], %[st], 3\n" \
    "s_mulk_i32 %[st], 5120\ns_add_i32 %[st], %[st], %[sb]\ns_mov_b32 exec_lo, 0xfffff\ns_mov_b32 exec_hi, 0xfffff\n" \
    "buffer_load_dwordx4 v[200:203], %[vo], %[rs], %[st] offen offset:0 sc1\nbuffer_load_dwordx4 v[204:207], %[vo], %[rs], %[st] offen offset:640 sc1\n" \
    "buffer_load_dwordx4 v[208:211], %[vo], %[rs], %[st] offen offset:1280 sc1\nbuffer_load_dwordx4 v[212:215], %[vo], %[rs], %[st] offen offset:1920 sc1\n" \
    "s_mov_b64 exec, -1\n"
#define CLOB_GEMM1 "v31", "v32", "v33", "v34", "v35", "v36", "v37", "v38", "v39", "v40", "v41", "v42", "v43", "v44", "v45", "v46", "v47", "v48", "v49", "v50", "v51", "v52", "v53", "v54", "v55", "v56", "v57", "v58", "v59", "v60", "v61", "v62", "v63", "v64", "v65", "v66", "v67", "v68", "v69", "v70", "v71"
#define ASM_QFIN \
    ".Lw_%=:\nds_read_b32 v104, %[xs]\nds_read_b128 v[64:67], %[e0] offset:0\nds_read_b128 v[84:87], %[e1] offset:0\n" \
    "ds_read_b128 v[68:71], %[e0] offset:1024\nds_read_b128 v[88:91], %[e1] offset:1024\nds_read_b128 v[72:75], %[e0] offset:2048\n" \
    "ds_read_b128 v[92:95], %[e1] offset:2048\nds_read_b128 v[76:79], %[e0] offset:3072\nds_read_b128 v[96:99], %[e1] offset:3072\n" \
    "ds_read_b128 v[80:83], %[e0] offset:4096\nds_read_b128 v[100:103], %[e1] offset:4096\ns_waitcnt lgkmcnt(10)\nv_readfirstlane_b32 %[st], v104\n" \
    "s_add_i32 %[cap], %[cap], -1\ns_cmp_le_i32 %[cap], 0\ns_cbranch_scc1 .Lg_%=\ns_cmp_ge_u32 %[st], 2\ns_cbranch_scc1 .Lg_%=\n.Ls_%=:\n" \
    "ds_read_b32 v104, %[xs]\ns_waitcnt lgkmcnt(0)\nv_readfirstlane_b32 %[st], v104\ns_add_i32 %[cap], %[cap], -1\ns_cmp_le_i32 %[cap], 0\n" \
    "s_cbranch_scc1 .Lw_%=\ns_cmp_ge_u32 %[st], 2\ns_cbranch_scc0 .Ls_%=\ns_branch .Lw_%=\n.Lg_%=:\nv_mov_b32 v104, 1\ns_waitcnt lgkmcnt(8)\n" \
    "v_pk_add_f32 v[64:65], v[64:65], v[84:85]\nv_pk_add_f32 v[66:67], v[66:67], v[86:87]\nv_cvt_pk_bf16_f32 v64, v64, v65\n" \
    "v_cvt_pk_bf16_f32 v65, v66, v67\nds_write_b64 %[q0], v[64:65]\ns_waitcnt lgkmcnt(7)\nv_pk_add_f32 v[68:69], v[68:69], v[88:89]\n" \
    "v_pk_add_f32 v[70:71], v[70:71], v[90:91]\nv_cvt_pk_bf16_f32 v68, v68, v69\nv_cvt_pk_bf16_f32 v69, v70, v71\nds_write_b64 %[q1], v[68:69]\n" \
    "s_waitcnt lgkmcnt(6)\nv_pk_add_f32 v[72:73], v[72:73], v[92:93]\nv_pk_add_f32 v[74:75], v[74:75], v[94:95]\nv_cvt_pk_bf16_f32 v72, v72, v73\n" \
    "v_cvt_pk_bf16_f32 v73, v74, v75\nds_write_b64 %[q2], v[72:73]\ns_waitcnt lgkmcnt(5)\nv_pk_add_f32 v[76:77], v[76:77], v[96:97]\n" \
    "v_pk_add_f32 v[78:79], v[78:79], v[98:99]\nv_cvt_pk_bf16_f32 v76, v76, v77\nv_cvt_pk_bf16_f32 v77, v78, v79\nds_write_b64 %[q3], v[76:77]\n" \
    "s_waitcnt lgkmcnt(4)\nv_pk_add_f32 v[80:81], v[80:81], v[100:101]\nv_pk_add_f32 v[82:83], v[82:83], v[102:103]\nv_cvt_pk_bf16_f32 v80, v80, v81\n" \
    "v_cvt_pk_bf16_f32 v81, v82, v83\nds_write_b64 %[q4], v[80:81]\ns_mov_b64 exec, 1\nds_add_u32 %[qd], v104\ns_mov_b64 exec, -1\n"
#define CLOB_QFIN "v64", "v65", "v66", "v67", "v68", "v69", "v70", "v71", "v72", "v73", "v74", "v75", "v76", "v77", "v78", "v79", "v80", "v81", "v82", "v83", "v84", "v85", "v86", "v87", "v88", "v89", "v90", "v91", "v92", "v93", "v94", "v95", "v96", "v97", "v98", "v99", "v100", "v101", "v102", "v103", "v104"
#define ASM_PRE \
    "ds_read_b64_tr_b16 v[136:137], %[wb1] offset:10240\nds_read_b64_tr_b16 v[138:139], %[wb2] offset:10240\n" \
    "ds_read_b64_tr_b16 v[140:141], %[wb1] offset:10272\nds_read_b64_tr_b16 v[142:143], %[wb2] offset:10272\n" \
    "ds_read_b64_tr_b16 v[144:145], %[wb1] offset:10304\nds_read_b64_tr_b16 v[146:147], %[wb2] offset:10304\n" \
    "ds_read_b64_tr_b16 v[148:149], %[wb1] offset:10336\nds_read_b64_tr_b16 v[150:151], %[wb2] offset:10336\n" \
    "ds_read_b64_tr_b16 v[152:153], %[wb1] offset:10368\nds_read_b64_tr_b16 v[154:155], %[wb2] offset:10368\n" \
    "ds_read_b64_tr_b16 v[156:157], %[wb1] offset:15360\nds_read_b64_tr_b16 v[158:159], %[wb2] offset:15360\n" \
    "ds_read_b64_tr_b16 v[160:161], %[wb1] offset:15392\nds_read_b64_tr_b16 v[162:163], %[wb2] offset:15392\n" \
    "ds_read_b64_tr_b16 v[164:165], %[wb1] offset:15424\nds_read_b64_tr_b16 v[166:167], %[wb2] offset:15424\n" \
    "ds_read_b64_tr_b16 v[168:169], %[wb1] offset:15456\nds_read_b64_tr_b16 v[170:171], %[wb2] offset:15456\n" \
    "ds_read_b64_tr_b16 v[172:173], %[wb1] offset:15488\nds_read_b64_tr_b16 v[174:175], %[wb2] offset:15488\n" \
    "ds_read_b64_tr_b16 v[176:177], %[wb1] offset:20480\nds_read_b64_tr_b16 v[178:179], %[wb2] offset:20480\n" \
    "ds_read_b64_tr_b16 v[180:181], %[wb1] offset:20512\nds_read_b64_tr_b16 v[182:183], %[wb2] offset:20512\n" \
    "ds_read_b64_tr_b16 v[184:185], %[wb1] offset:20544\nds_read_b64_tr_b16 v[186:187], %[wb2] offset:20544\n" \
    "ds_read_b64_tr_b16 v[188:189], %[wb1] offset:20576\nds_read_b64_tr_b16 v[190:191], %[wb2] offset:20576\n" \
    "ds_read_b64_tr_b16 v[192:193], %[wb1] offset:20608\nds_read_b64_tr_b16 v[194:195], %[wb2] offset:20608\n" \
    "ds_read_b64_tr_b16 v[196:197], %[wb1] offset:25600\nds_read_b64_tr_b16 v[198:199], %[wb2] offset:25600\n" \
    "ds_read_b64_tr_b16 v[200:201], %[wb1] offset:25632\nds_read_b64_tr_b16 v[202:203], %[wb2] offset:25632\n" \
    "ds_read_b64_tr_b16 v[204:205], %[wb1] offset:25664\nds_read_b64_tr_b16 v[206:207], %[wb2] offset:25664\n" \
    "ds_read_b64_tr_b16 v[208:209], %[wb1] offset:25696\nds_read_b64_tr_b16 v[210:211], %[wb2] offset:25696\n" \
    "ds_read_b64_tr_b16 v[212:213], %[wb1] offset:25728\nds_read_b64_tr_b16 v[214:215], %[wb2] offset:25728\n" \
    "ds_read_b64_tr_b16 v[216:217], %[wb1] offset:30720\nds_read_b64_tr_b16 v[218:219], %[wb2] offset:30720\n" \
    "ds_read_b64_tr_b16 v[220:221], %[wb1] offset:30752\nds_read_b64_tr_b16 v[222:223], %[wb2] offset:30752\n" \
    "ds_read_b64_tr_b16 v[224:225], %[wb1] offset:30784\nds_read_b64_tr_b16 v[226:227], %[wb2] offset:30784\n" \
    "ds_read_b64_tr_b16 v[228:229], %[wb1] offset:30816\nds_read_b64_tr_b16 v[230:231], %[wb2] offset:30816\n" \
    "ds_read_b64_tr_b16 v[232:233], %[wb1] offset:30848\nds_read_b64_tr_b16 v[234:235], %[wb2] offset:30848\n" \
    "ds_read_b64_tr_b16 v[236:237], %[wb1] offset:35840\nds_read_b64_tr_b16 v[238:239], %[wb2] offset:35840\n" \
    "ds_read_b64_tr_b16 v[240:241], %[wb1] offset:35872\nds_read_b64_tr_b16 v[242:243], %[wb2] offset:35872\n" \
    "ds_read_b64_tr_b16 v[244:245], %[wb1] offset:35904\nds_read_b64_tr_b16 v[246:247], %[wb2] offset:35904\n" \
    "ds_read_b64_tr_b16 v[248:249], %[wb1] offset:35936\nds_read_b64_tr_b16 v[250:251], %[wb2] offset:35936\n" \
    "ds_read_b64_tr_b16 v[252:253], %[wb1] offset:35968\nds_read_b64_tr_b16 v[254:255], %[wb2] offset:35968\ns_waitcnt lgkmcnt(0)\n"
#define ASM_GEMM2 \
    "buffer_load_dwordx4 v[56:59], %[ob], %[rs], %[so0] offen offset:0 sc1\nbuffer_load_dwordx4 v[60:63], %[ob], %[rs], %[so0] offen offset:1024 sc1\n" \
    "buffer_load_dwordx4 v[64:67], %[ob], %[rs], %[so0] offen offset:2048 sc1\nbuffer_load_dwordx4 v[68:71], %[ob], %[rs], %[so0] offen offset:3072 sc1\n" \
    "buffer_load_dwordx4 v[72:75], %[ob], %[rs], %[so1] offen offset:0 sc1\nbuffer_load_dwordx4 v[76:79], %[ob], %[rs], %[so1] offen offset:1024 sc1\n" \
    "buffer_load_dwordx4 v[80:83], %[ob], %[rs], %[so1] offen offset:2048 sc1\nbuffer_load_dwordx4 v[84:87], %[ob], %[rs], %[so1] offen offset:3072 sc1\n" \
    "buffer_load_dwordx2 v[88:89], %[ob2], %[rs], %[so0] offen offset:0 sc1\nbuffer_load_dwordx2 v[90:91], %[ob2], %[rs], %[so0] offen offset:128 sc1\n" \
    "buffer_load_dwordx2 v[92:93], %[ob2], %[rs], %[so0] offen offset:1024 sc1\nbuffer_load_dwordx2 v[94:95], %[ob2], %[rs], %[so0] offen offset:1152 sc1\n" \
    "ds_read_b64_tr_b16 v[96:97], %[wb1] offset:0\nds_read_b64_tr_b16 v[98:99], %[wb2] offset:0\nds_read_b64_tr_b16 v[100:101], %[wb1] offset:32\n" \
    "ds_read_b64_tr_b16 v[102:103], %[wb2] offset:32\nds_read_b64_tr_b16 v[104:105], %[wb1] offset:64\nds_read_b64_tr_b16 v[106:107], %[wb2] offset:64\n" \
    "ds_read_b64_tr_b16 v[108:109], %[wb1] offset:96\nds_read_b64_tr_b16 v[110:111], %[wb2] offset:96\nds_read_b64_tr_b16 v[112:113], %[wb1] offset:128\n" \
    "ds_read_b64_tr_b16 v[114:115], %[wb2] offset:128\nds_read_b64_tr_b16 v[116:117], %[wb1] offset:5120\n" \
    "ds_read_b64_tr_b16 v[118:119], %[wb2] offset:5120\nds_read_b64_tr_b16 v[120:121], %[wb1] offset:5152\n" \
    "ds_read_b64_tr_b16 v[122:123], %[wb2] offset:5152\nds_read_b64_tr_b16 v[124:125], %[wb1] offset:5184\n" \
    "ds_read_b64_tr_b16 v[126:127], %[wb2] offset:5184\nds_read_b64_tr_b16 v[128:129], %[wb1] offset:5216\n" \
    "ds_read_b64_tr_b16 v[130:131], %[wb2] offset:5216\nds_read_b64_tr_b16 v[132:133], %[wb1] offset:5248\n" \
    "ds_read_b64_tr_b16 v[134:135], %[wb2] offset:5248\ns_waitcnt vmcnt(11)\nv_max3_u32 v35, v56, v57, v58\nv_max_u32 v35, v35, v59\n" \
    "v_cmp_eq_u32 vcc, -1, v35\ns_cbranch_vccnz .Lr0_%=\n.Lf0_%=:\n" \
    "s_waitcnt lgkmcnt(0)\nv_mfma_f32_16x16x32_bf16 v[36:39], v[96:99], v[56:59], 0\nds_read_b64_tr_b16 v[96:97], %[wb1] offset:40960\n" \
    "ds_read_b64_tr_b16 v[98:99], %[wb2] offset:40960\ns_waitcnt vmcnt(10)\nv_max3_u32 v35, v60, v61, v62\n" \
    "v_mfma_f32_16x16x32_bf16 v[40:43], v[100:103], v[56:59], 0\nds_read_b64_tr_b16 v[100:101], %[wb1] offset:40992\n" \
    "ds_read_b64_tr_b16 v[102:103], %[wb2] offset:40992\nv_max_u32 v35, v35, v63\nv_mfma_f32_16x16x32_bf16 v[44:47], v[104:107], v[56:59], 0\n" \
    "ds_read_b64_tr_b16 v[104:105], %[wb1] offset:41024\nds_read_b64_tr_b16 v[106:107], %[wb2] offset:41024\nv_cmp_eq_u32 vcc, -1, v35\n" \
    "v_mfma_f32_16x16x32_bf16 v[48:51], v[108:111], v[56:59], 0\nds_read_b64_tr_b16 v[108:109], %[wb1] offset:41056\n" \
    "ds_read_b64_tr_b16 v[110:111], %[wb2] offset:41056\nv_mfma_f32_16x16x32_bf16 v[52:55], v[112:115], v[56:59], 0\n" \
    "ds_read_b64_tr_b16 v[112:113], %[wb1] offset:41088\nds_read_b64_tr_b16 v[114:115], %[wb2] offset:41088\ns_cbranch_vccnz .Lr1_%=\n.Lf1_%=:\n" \
    "v_mfma_f32_16x16x32_bf16 v[36:39], v[116:119], v[60:63], v[36:39]\nds_read_b64_tr_b16 v[116:117], %[wb1] offset:46080\n" \
    "ds_read_b64_tr_b16 v[118:119], %[wb2] offset:46080\ns_waitcnt vmcnt(9)\nv_max3_u32 v35, v64, v65, v66\n" \
    "v_mfma_f32_16x16x32_bf16 v[40:43], v[120:123], v[60:63], v[40:43]\nds_read_b64_tr_b16 v[120:121], %[wb1] offset:46112\n" \
    "ds_read_b64_tr_b16 v[122:123], %[wb2] offset:46112\nv_max_u32 v35, v35, v67\nv_mfma_f32_16x16x32_bf16 v[44:47], v[124:127], v[60:63], v[44:47]\n" \
    "ds_read_b64_tr_b16 v[124:125], %[wb1] offset:46144\nds_read_b64_tr_b16 v[126:127], %[wb2] offset:46144\nv_cmp_eq_u32 vcc, -1, v35\n" \
    "v_mfma_f32_16x16x32_bf16 v[48:51], v[128:131], v[60:63], v[48:51]\nds_read_b64_tr_b16 v[128:129], %[wb1] offset:46176\n" \
    "ds_read_b64_tr_b16 v[130:131], %[wb2] offset:46176\nv_mfma_f32_16x16x32_bf16 v[52:55], v[132:135], v[60:63], v[52:55]\n" \
    "ds_read_b64_tr_b16 v[132:133], %[wb1] offset:46208\nds_read_b64_tr_b16 v[134:135], %[wb2] offset:46208\ns_cbranch_vccnz .Lr2_%=\n.Lf2_%=:\n" \
    "v_mfma_f32_16x16x32_bf16 v[36:39], v[136:139], v[64:67], v[36:39]\ns_waitcnt vmcnt(8)\nv_max3_u32 v35, v68, v69, v70\n" \
    "v_mfma_f32_16x16x32_bf16 v[40:43], v[140:143], v[64:67], v[40:43]\nv_max_u32 v35, v35, v71\n" \
    "v_mfma_f32_16x16x32_bf16 v[44:47], v[144:147], v[64:67], v[44:47]\nv_cmp_eq_u32 vcc, -1, v35\n" \
    "v_mfma_f32_16x16x32_bf16 v[48:51], v[148:151], v[64:67], v[48:51]\nv_mfma_f32_16x16x32_bf16 v[52:55], v[152:155], v[64:67], v[52:55]\n" \
    "s_cbranch_vccnz .Lr3_%=\n.Lf3_%=:\nv_mfma_f32_16x16x32_bf16 v[36:39], v[156:159], v[68:71], v[36:39]\ns_waitcnt vmcnt(7)\n" \
    "v_max3_u32 v35, v72, v73, v74\nv_mfma_f32_16x16x32_bf16 v[40:43], v[160:163], v[68:71], v[40:43]\nv_max_u32 v35, v35, v75\n" \
    "v_mfma_f32_16x16x32_bf16 v[44:47], v[164:167], v[68:71], v[44:47]\nv_cmp_eq_u32 vcc, -1, v35\n" \
    "v_mfma_f32_16x16x32_bf16 v[48:51], v[168:171], v[68:71], v[48:51]\nv_mfma_f32_16x16x32_bf16 v[52:55], v[172:175], v[68:71], v[52:55]\n" \
    "s_cbranch_vccnz .Lr4_%=\n.Lf4_%=:\n" \
    "v_mfma_f32_16x16x32_bf16 v[36:39], v[176:179], v[72:75], v[36:39]\ns_waitcnt vmcnt(6)\nv_max3_u32 v35, v76, v77, v78\n" \
    "v_mfma_f32_16x16x32_bf16 v[40:43], v[180:183], v[72:75], v[40:43]\nv_max_u32 v35, v35, v79\n" \
    "v_mfma_f32_16x16x32_bf16 v[44:47], v[184:187], v[72:75], v[44:47]\nv_cmp_eq_u32 vcc, -1, v35\n" \
    "v_mfma_f32_16x16x32_bf16 v[48:51], v[188:191], v[72:75], v[48:51]\nv_mfma_f32_16x16x32_bf16 v[52:55], v[192:195], v[72:75], v[52:55]\n" \
    "s_cbranch_vccnz .Lr5_%=\n.Lf5_%=:\nv_mfma_f32_16x16x32_bf16 v[36:39], v[196:199], v[76:79], v[36:39]\ns_waitcnt vmcnt(5)\n" \
    "v_max3_u32 v35, v80, v81, v82\nv_mfma_f32_16x16x32_bf16 v[40:43], v[200:203], v[76:79], v[40:43]\nv_max_u32 v35, v35, v83\n" \
    "v_mfma_f32_16x16x32_bf16 v[44:47], v[204:207], v[76:79], v[44:47]\nv_cmp_eq_u32 vcc, -1, v35\n" \
    "v_mfma_f32_16x16x32_bf16 v[48:51], v[208:211], v[76:79], v[48:51]\nv_mfma_f32_16x16x32_bf16 v[52:55], v[212:215], v[76:79], v[52:55]\n" \
    "s_cbranch_vccnz .Lr6_%=\n.Lf6_%=:\nv_mfma_f32_16x16x32_bf16 v[36:39], v[216:219], v[80:83], v[36:39]\ns_waitcnt vmcnt(4)\n" \
    "v_max3_u32 v35, v84, v85, v86\nv_mfma_f32_16x16x32_bf16 v[40:43], v[220:223], v[80:83], v[40:43]\nv_max_u32 v35, v35, v87\n" \
    "v_mfma_f32_16x16x32_bf16 v[44:47], v[224:227], v[80:83], v[44:47]\nv_cmp_eq_u32 vcc, -1, v35\n" \
    "v_mfma_f32_16x16x32_bf16 v[48:51], v[228:231], v[80:83], v[48:51]\nv_mfma_f32_16x16x32_bf16 v[52:55], v[232:235], v[80:83], v[52:55]\n" \
    "s_cbranch_vccnz .Lr7_%=\n.Lf7_%=:\nv_mfma_f32_16x16x32_bf16 v[36:39], v[236:239], v[84:87], v[36:39]\ns_waitcnt vmcnt(2)\n" \
    "v_max3_u32 v35, v88, v89, v90\nv_mfma_f32_16x16x32_bf16 v[40:43], v[240:243], v[84:87], v[40:43]\nv_max_u32 v35, v35, v91\n" \
    "v_mfma_f32_16x16x32_bf16 v[44:47], v[244:247], v[84:87], v[44:47]\nv_cmp_eq_u32 vcc, -1, v35\n" \
    "v_mfma_f32_16x16x32_bf16 v[48:51], v[248:251], v[84:87], v[48:51]\nv_mfma_f32_16x16x32_bf16 v[52:55], v[252:255], v[84:87], v[52:55]\n" \
    "s_cbranch_vccnz .Lr8_%=\n.Lf8_%=:\ns_waitcnt lgkmcnt(0)\nv_mfma_f32_16x16x32_bf16 v[36:39], v[96:99], v[88:91], v[36:39]\ns_waitcnt vmcnt(0)\n" \
    "v_max3_u32 v35, v92, v93, v94\nv_mfma_f32_16x16x32_bf16 v[40:43], v[100:103], v[88:91], v[40:43]\nv_max_u32 v35, v35, v95\n" \
    "v_mfma_f32_16x16x32_bf16 v[44:47], v[104:107], v[88:91], v[44:47]\nv_cmp_eq_u32 vcc, -1, v35\n" \
    "v_mfma_f32_16x16x32_bf16 v[48:51], v[108:111], v[88:91], v[48:51]\nv_mfma_f32_16x16x32_bf16 v[52:55], v[112:115], v[88:91], v[52:55]\n" \
    "s_cbranch_vccnz .Lr9_%=\n.Lf9_%=:\n" \
    "v_mfma_f32_16x16x32_bf16 v[36:39], v[116:119], v[92:95], v[36:39]\nv_mfma_f32_16x16x32_bf16 v[40:43], v[120:123], v[92:95], v[40:43]\n" \
    "v_mfma_f32_16x16x32_bf16 v[44:47], v[124:127], v[92:95], v[44:47]\nv_mfma_f32_16x16x32_bf16 v[48:51], v[128:131], v[92:95], v[48:51]\n" \
    "v_mfma_f32_16x16x32_bf16 v[52:55], v[132:135], v[92:95], v[52:55]\ns_branch .Ldone_%=\n.Lr0_%=:\ns_add_i32 %[cap], %[cap], -1\n" \
    "s_cmp_le_i32 %[cap], 0\ns_cbranch_scc1 .Lf0_%=\ns_mov_b32 %[ret], 0\ns_waitcnt vmcnt(0)\ns_branch .Ll0_%=\n.Lr1_%=:\ns_add_i32 %[cap], %[cap], -1\n" \
    "s_cmp_le_i32 %[cap], 0\ns_cbranch_scc1 .Lf1_%=\ns_mov_b32 %[ret], 1\ns_waitcnt vmcnt(0)\ns_branch .Ll1_%=\n.Lr2_%=:\ns_add_i32 %[cap], %[cap], -1\n" \
    "s_cmp_le_i32 %[cap], 0\ns_cbranch_scc1 .Lf2_%=\ns_mov_b32 %[ret], 2\ns_waitcnt vmcnt(0)\ns_branch .Ll2_%=\n.Lr3_%=:\ns_add_i32 %[cap], %[cap], -1\n" \
    "s_cmp_le_i32 %[cap], 0\ns_cbranch_scc1 .Lf3_%=\ns_mov_b32 %[ret], 3\ns_waitcnt vmcnt(0)\ns_branch .Ll3_%=\n.Lr4_%=:\ns_add_i32 %[cap], %[cap], -1\n" \
    "s_cmp_le_i32 %[cap], 0\ns_cbranch_scc1 .Lf4_%=\ns_mov_b32 %[ret], 4\ns_waitcnt vmcnt(0)\ns_branch .Ll4_%=\n.Lr5_%=:\ns_add_i32 %[cap], %[cap], -1\n" \
    "s_cmp_le_i32 %[cap], 0\ns_cbranch_scc1 .Lf5_%=\ns_mov_b32 %[ret], 5\ns_waitcnt vmcnt(0)\ns_branch .Ll5_%=\n.Lr6_%=:\ns_add_i32 %[cap], %[cap], -1\n" \
    "s_cmp_le_i32 %[cap], 0\ns_cbranch_scc1 .Lf6_%=\ns_mov_b32 %[ret], 6\ns_waitcnt vmcnt(0)\ns_branch .Ll6_%=\n.Lr7_%=:\ns_add_i32 %[cap], %[cap], -1\n" \
    "s_cmp_le_i32 %[cap], 0\ns_cbranch_scc1 .Lf7_%=\ns_mov_b32 %[ret], 7\ns_waitcnt vmcnt(0)\ns_branch .Ll7_%=\n.Lr8_%=:\ns_add_i32 %[cap], %[cap], -1\n" \
    "s_cmp_le_i32 %[cap], 0\ns_cbranch_scc1 .Lf8_%=\ns_mov_b32 %[ret], 8\ns_waitcnt vmcnt(0)\ns_branch .Ll8_%=\n.Lr9_%=:\ns_add_i32 %[cap], %[cap], -1\n" \
    "s_cmp_le_i32 %[cap], 0\ns_cbranch_scc1 .Lf9_%=\ns_mov_b32 %[ret], 9\ns_waitcnt vmcnt(0)\ns_branch .Ll9_%=\n.Ll0_%=:\n" \
    "buffer_load_dwordx4 v[56:59], %[ob], %[rs], %[so0] offen offset:0 sc1\n.Ll1_%=:\n" \
    "buffer_load_dwordx4 v[60:63], %[ob], %[rs], %[so0] offen offset:1024 sc1\n.Ll2_%=:\n" \
    "buffer_load_dwordx4 v[64:67], %[ob], %[rs], %[so0] offen offset:2048 sc1\n.Ll3_%=:\n" \
    "buffer_load_dwordx4 v[68:71], %[ob], %[rs], %[so0] offen offset:3072 sc1\n.Ll4_%=:\n" \
    "buffer_load_dwordx4 v[72:75], %[ob], %[rs], %[so1] offen offset:0 sc1\n.Ll5_%=:\n" \
    "buffer_load_dwordx4 v[76:79], %[ob], %[rs], %[so1] offen offset:1024 sc1\n.Ll6_%=:\n" \
    "buffer_load_dwordx4 v[80:83], %[ob], %[rs], %[so1] offen offset:2048 sc1\n.Ll7_%=:\n" \
    "buffer_load_dwordx4 v[84:87], %[ob], %[rs], %[so1] offen offset:3072 sc1\n.Ll8_%=:\n" \
    "buffer_load_dwordx2 v[88:89], %[ob2], %[rs], %[so0] offen offset:0 sc1\nbuffer_load_dwordx2 v[90:91], %[ob2], %[rs], %[so0] offen offset:128 sc1\n" \
    ".Ll9_%=:\nbuffer_load_dwordx2 v[92:93], %[ob2], %[rs], %[so0] offen offset:1024 sc1\n" \
    "buffer_load_dwordx2 v[94:95], %[ob2], %[rs], %[so0] offen offset:1152 sc1\ns_cmp_eq_u32 %[ret], 0\ns_cbranch_scc1 .Lc0_%=\ns_cmp_eq_u32 %[ret], 1\n" \
    "s_cbranch_scc1 .Lc1_%=\ns_cmp_eq_u32 %[ret], 2\ns_cbranch_scc1 .Lc2_%=\ns_cmp_eq_u32 %[ret], 3\ns_cbranch_scc1 .Lc3_%=\ns_cmp_eq_u32 %[ret], 4\n" \
    "s_cbranch_scc1 .Lc4_%=\ns_cmp_eq_u32 %[ret], 5\ns_cbranch_scc1 .Lc5_%=\ns_cmp_eq_u32 %[ret], 6\ns_cbranch_scc1 .Lc6_%=\ns_cmp_eq_u32 %[ret], 7\n" \
    "s_cbranch_scc1 .Lc7_%=\ns_cmp_eq_u32 %[ret], 8\ns_cbranch_scc1 .Lc8_%=\ns_cmp_eq_u32 %[ret], 9\ns_cbranch_scc1 .Lc9_%=\n.Lc0_%=:\n" \
    "s_waitcnt vmcnt(11)\nv_max3_u32 v35, v56, v57, v58\nv_max_u32 v35, v35, v59\nv_cmp_eq_u32 vcc, -1, v35\ns_cbranch_vccnz .Lr0_%=\ns_branch .Lf0_%=\n" \
    ".Lc1_%=:\ns_waitcnt vmcnt(10)\nv_max3_u32 v35, v60, v61, v62\nv_max_u32 v35, v35, v63\nv_cmp_eq_u32 vcc, -1, v35\ns_cbranch_vccnz .Lr1_%=\n" \
    "s_branch .Lf1_%=\n.Lc2_%=:\ns_waitcnt vmcnt(9)\nv_max3_u32 v35, v64, v65, v66\nv_max_u32 v35, v35, v67\nv_cmp_eq_u32 vcc, -1, v35\n" \
    "s_cbranch_vccnz .Lr2_%=\ns_branch .Lf2_%=\n.Lc3_%=:\ns_waitcnt vmcnt(8)\nv_max3_u32 v35, v68, v69, v70\nv_max_u32 v35, v35, v71\n" \
    "v_cmp_eq_u32 vcc, -1, v35\ns_cbranch_vccnz .Lr3_%=\ns_branch .Lf3_%=\n.Lc4_%=:\ns_waitcnt vmcnt(7)\nv_max3_u32 v35, v72, v73, v74\n" \
    "v_max_u32 v35, v35, v75\nv_cmp_eq_u32 vcc, -1, v35\ns_cbranch_vccnz .Lr4_%=\ns_branch .Lf4_%=\n.Lc5_%=:\ns_waitcnt vmcnt(6)\n" \
    "v_max3_u32 v35, v76, v77, v78\nv_max_u32 v35, v35, v79\nv_cmp_eq_u32 vcc, -1, v35\ns_cbranch_vccnz .Lr5_%=\ns_branch .Lf5_%=\n.Lc6_%=:\n" \
    "s_waitcnt vmcnt(5)\nv_max3_u32 v35, v80, v81, v82\nv_max_u32 v35, v35, v83\nv_cmp_eq_u32 vcc, -1, v35\ns_cbranch_vccnz .Lr6_%=\ns_branch .Lf6_%=\n" \
    ".Lc7_%=:\ns_waitcnt vmcnt(4)\nv_max3_u32 v35, v84, v85, v86\nv_max_u32 v35, v35, v87\nv_cmp_eq_u32 vcc, -1, v35\ns_cbranch_vccnz .Lr7_%=\n" \
    "s_branch .Lf7_%=\n.Lc8_%=:\ns_waitcnt vmcnt(2)\nv_max3_u32 v35, v88, v89, v90\nv_max_u32 v35, v35, v91\nv_cmp_eq_u32 vcc, -1, v35\n" \
    "s_cbranch_vccnz .Lr8_%=\ns_branch .Lf8_%=\n.Lc9_%=:\ns_waitcnt vmcnt(0)\nv_max3_u32 v35, v92, v93, v94\nv_max_u32 v35, v35, v95\n" \
    "v_cmp_eq_u32 vcc, -1, v35\ns_cbranch_vccnz .Lr9_%=\ns_branch .Lf9_%=\n.Ldone_%=:\nv_mov_b32 v35, 1\ns_nop 2\nds_write_b128 %[sa], v[36:39] offset:0\n" \
    "s_nop 0\nds_write_b128 %[sa], v[40:43] offset:64\ns_nop 0\nds_write_b128 %[sa], v[44:47] offset:128\ns_nop 0\n" \
    "ds_write_b128 %[sa], v[48:51] offset:192\ns_nop 0\nds_write_b128 %[sa], v[52:55] offset:256\ns_mov_b64 exec, 1\nds_add_u32 %[ep], v35\n" \
    "s_mov_b64 exec, -1\n"
#define ASM_EVLOAD \
    "ds_read_b64_tr_b16 v[96:97], %[wb1] offset:0\nds_read_b64_tr_b16 v[98:99], %[wb2] offset:0\nds_read_b64_tr_b16 v[100:101], %[wb1] offset:32\n" \
    "ds_read_b64_tr_b16 v[102:103], %[wb2] offset:32\nds_read_b64_tr_b16 v[104:105], %[wb1] offset:64\nds_read_b64_tr_b16 v[106:107], %[wb2] offset:64\n" \
    "ds_read_b64_tr_b16 v[108:109], %[wb1] offset:96\nds_read_b64_tr_b16 v[110:111], %[wb2] offset:96\nds_read_b64_tr_b16 v[112:113], %[wb1] offset:128\n" \
    "ds_read_b64_tr_b16 v[114:115], %[wb2] offset:128\nds_read_b64_tr_b16 v[116:117], %[wb1] offset:5120\n" \
    "ds_read_b64_tr_b16 v[118:119], %[wb2] offset:5120\nds_read_b64_tr_b16 v[120:121], %[wb1] offset:5152\n" \
    "ds_read_b64_tr_b16 v[122:123], %[wb2] offset:5152\nds_read_b64_tr_b16 v[124:125], %[wb1] offset:5184\n" \
    "ds_read_b64_tr_b16 v[126:127], %[wb2] offset:5184\nds_read_b64_tr_b16 v[128:129], %[wb1] offset:5216\n" \
    "ds_read_b64_tr_b16 v[130:131], %[wb2] offset:5216\nds_read_b64_tr_b16 v[132:133], %[wb1] offset:5248\n" \
    "ds_read_b64_tr_b16 v[134:135], %[wb2] offset:5248\n.Ll_%=:\nbuffer_load_dwordx4 v[56:59], %[ob], %[rs], %[so0] offen offset:0 sc1\n" \
    "buffer_load_dwordx4 v[60:63], %[ob], %[rs], %[so0] offen offset:1024 sc1\nbuffer_load_dwordx4 v[64:67], %[ob], %[rs], %[so0] offen offset:2048 sc1\n" \
    "buffer_load_dwordx4 v[68:71], %[ob], %[rs], %[so0] offen offset:3072 sc1\nbuffer_load_dwordx4 v[72:75], %[ob], %[rs], %[so1] offen offset:0 sc1\n" \
    "buffer_load_dwordx4 v[76:79], %[ob], %[rs], %[so1] offen offset:1024 sc1\nbuffer_load_dwordx4 v[80:83], %[ob], %[rs], %[so1] offen offset:2048 sc1\n" \
    "buffer_load_dwordx4 v[84:87], %[ob], %[rs], %[so1] offen offset:3072 sc1\nbuffer_load_dwordx2 v[88:89], %[ob2], %[rs], %[so0] offen offset:0 sc1\n" \
    "buffer_load_dwordx2 v[90:91], %[ob2], %[rs], %[so0] offen offset:128 sc1\nbuffer_load_dwordx2 v[92:93], %[ob2], %[rs], %[so0] offen offset:1024 sc1\n" \
    "buffer_load_dwordx2 v[94:95], %[ob2], %[rs], %[so0] offen offset:1152 sc1\ns_waitcnt vmcnt(0)\nv_max_u32 v35, v56, v57\n" \
    "v_max3_u32 v35, v35, v58, v59\nv_max3_u32 v35, v35, v60, v61\nv_max3_u32 v35, v35, v62, v63\nv_max3_u32 v35, v35, v64, v65\n" \
    "v_max3_u32 v35, v35, v66, v67\nv_max3_u32 v35, v35, v68, v69\nv_max3_u32 v35, v35, v70, v71\nv_max3_u32 v35, v35, v72, v73\n" \
    "v_max3_u32 v35, v35, v74, v75\nv_max3_u32 v35, v35, v76, v77\nv_max3_u32 v35, v35, v78, v79\nv_max3_u32 v35, v35, v80, v81\n" \
    "v_max3_u32 v35, v35, v82, v83\nv_max3_u32 v35, v35, v84, v85\nv_max3_u32 v35, v35, v86, v87\nv_max3_u32 v35, v35, v88, v89\n" \
    "v_max3_u32 v35, v35, v90, v91\nv_max3_u32 v35, v35, v92, v93\nv_max3_u32 v35, v35, v94, v95\nv_cmp_eq_u32 vcc, -1, v35\ns_add_i32 %[cap], %[cap], -1\n" \
    "s_cmp_le_i32 %[cap], 0\ns_cbranch_scc1 .Ld_%=\ns_cbranch_vccz .Ld_%=\ns_sleep 8\ns_branch .Ll_%=\n.Ld_%=:\ns_waitcnt lgkmcnt(0)\n"
#define ASM_PROJ2 \
    "v_mfma_f32_16x16x32_bf16 v[36:39], v[96:99], v[56:59], 0\nds_read_b64_tr_b16 v[96:97], %[wb1] offset:40960\n" \
    "ds_read_b64_tr_b16 v[98:99], %[wb2] offset:40960\nv_mfma_f32_16x16x32_bf16 v[40:43], v[100:103], v[56:59], 0\n" \
    "ds_read_b64_tr_b16 v[100:101], %[wb1] offset:40992\nds_read_b64_tr_b16 v[102:103], %[wb2] offset:40992\n" \
    "v_mfma_f32_16x16x32_bf16 v[44:47], v[104:107], v[56:59], 0\nds_read_b64_tr_b16 v[104:105], %[wb1] offset:41024\n" \
    "ds_read_b64_tr_b16 v[106:107], %[wb2] offset:41024\nv_mfma_f32_16x16x32_bf16 v[48:51], v[108:111], v[56:59], 0\n" \
    "ds_read_b64_tr_b16 v[108:109], %[wb1] offset:41056\nds_read_b64_tr_b16 v[110:111], %[wb2] offset:41056\n" \
    "v_mfma_f32_16x16x32_bf16 v[52:55], v[112:115], v[56:59], 0\nds_read_b64_tr_b16 v[112:113], %[wb1] offset:41088\n" \
    "ds_read_b64_tr_b16 v[114:115], %[wb2] offset:41088\nv_mfma_f32_16x16x32_bf16 v[36:39], v[116:119], v[60:63], v[36:39]\n" \
    "ds_read_b64_tr_b16 v[116:117], %[wb1] offset:46080\nds_read_b64_tr_b16 v[118:119], %[wb2] offset:46080\n" \
    "v_mfma_f32_16x16x32_bf16 v[40:43], v[120:123], v[60:63], v[40:43]\nds_read_b64_tr_b16 v[120:121], %[wb1] offset:46112\n" \
    "ds_read_b64_tr_b16 v[122:123], %[wb2] offset:46112\nv_mfma_f32_16x16x32_bf16 v[44:47], v[124:127], v[60:63], v[44:47]\n" \
    "ds_read_b64_tr_b16 v[124:125], %[wb1] offset:46144\nds_read_b64_tr_b16 v[126:127], %[wb2] offset:46144\n" \
    "v_mfma_f32_16x16x32_bf16 v[48:51], v[128:131], v[60:63], v[48:51]\nds_read_b64_tr_b16 v[128:129], %[wb1] offset:46176\n" \
    "ds_read_b64_tr_b16 v[130:131], %[wb2] offset:46176\nv_mfma_f32_16x16x32_bf16 v[52:55], v[132:135], v[60:63], v[52:55]\n" \
    "ds_read_b64_tr_b16 v[132:133], %[wb1] offset:46208\nds_read_b64_tr_b16 v[134:135], %[wb2] offset:46208\n" \
    "v_mfma_f32_16x16x32_bf16 v[36:39], v[136:139], v[64:67], v[36:39]\nv_mfma_f32_16x16x32_bf16 v[40:43], v[140:143], v[64:67], v[40:43]\n" \
    "v_mfma_f32_16x16x32_bf16 v[44:47], v[144:147], v[64:67], v[44:47]\nv_mfma_f32_16x16x32_bf16 v[48:51], v[148:151], v[64:67], v[48:51]\n" \
    "v_mfma_f32_16x16x32_bf16 v[52:55], v[152:155], v[64:67], v[52:55]\nv_mfma_f32_16x16x32_bf16 v[36:39], v[156:159], v[68:71], v[36:39]\n" \
    "v_mfma_f32_16x16x32_bf16 v[40:43], v[160:163], v[68:71], v[40:43]\nv_mfma_f32_16x16x32_bf16 v[44:47], v[164:167], v[68:71], v[44:47]\n" \
    "v_mfma_f32_16x16x32_bf16 v[48:51], v[168:171], v[68:71], v[48:51]\nv_mfma_f32_16x16x32_bf16 v[52:55], v[172:175], v[68:71], v[52:55]\n" \
    "v_mfma_f32_16x16x32_bf16 v[36:39], v[176:179], v[72:75], v[36:39]\nv_mfma_f32_16x16x32_bf16 v[40:43], v[180:183], v[72:75], v[40:43]\n" \
    "v_mfma_f32_16x16x32_bf16 v[44:47], v[184:187], v[72:75], v[44:47]\nv_mfma_f32_16x16x32_bf16 v[48:51], v[188:191], v[72:75], v[48:51]\n" \
    "v_mfma_f32_16x16x32_bf16 v[52:55], v[192:195], v[72:75], v[52:55]\nv_mfma_f32_16x16x32_bf16 v[36:39], v[196:199], v[76:79], v[36:39]\n" \
    "v_mfma_f32_16x16x32_bf16 v[40:43], v[200:203], v[76:79], v[40:43]\nv_mfma_f32_16x16x32_bf16 v[44:47], v[204:207], v[76:79], v[44:47]\n" \
    "v_mfma_f32_16x16x32_bf16 v[48:51], v[208:211], v[76:79], v[48:51]\nv_mfma_f32_16x16x32_bf16 v[52:55], v[212:215], v[76:79], v[52:55]\n" \
    "ds_read_b64 v[34:35], %[fin]\nv_mfma_f32_16x16x32_bf16 v[36:39], v[216:219], v[80:83], v[36:39]\n" \
    "v_mfma_f32_16x16x32_bf16 v[40:43], v[220:223], v[80:83], v[40:43]\nv_mfma_f32_16x16x32_bf16 v[44:47], v[224:227], v[80:83], v[44:47]\n" \
    "v_mfma_f32_16x16x32_bf16 v[48:51], v[228:231], v[80:83], v[48:51]\nv_mfma_f32_16x16x32_bf16 v[52:55], v[232:235], v[80:83], v[52:55]\n" \
    "v_mfma_f32_16x16x32_bf16 v[36:39], v[236:239], v[84:87], v[36:39]\nv_mfma_f32_16x16x32_bf16 v[40:43], v[240:243], v[84:87], v[40:43]\n" \
    "v_mfma_f32_16x16x32_bf16 v[44:47], v[244:247], v[84:87], v[44:47]\nv_mfma_f32_16x16x32_bf16 v[48:51], v[248:251], v[84:87], v[48:51]\n" \
    "v_mfma_f32_16x16x32_bf16 v[52:55], v[252:255], v[84:87], v[52:55]\n.Lv_%=:\ns_waitcnt lgkmcnt(0)\nv_readfirstlane_b32 %[fo], v34\n" \
    "v_readfirstlane_b32 %[t0], v35\ns_add_i32 %[cap], %[cap], -1\ns_cmp_le_i32 %[cap], 0\ns_cbranch_scc1 .Lu_%=\ns_bitcmp1_b32 %[fo], 7\n" \
    "s_cbranch_scc1 .Lu_%=\nds_read_b64 v[34:35], %[fin]\ns_branch .Lv_%=\n.Lu_%=:\ns_lshr_b32 %[fo], %[fo], 8\ns_cbranch_scc1 .Lx_%=\n.Lt_%=:\n" \
    "s_memrealtime %[tm]\ns_waitcnt lgkmcnt(0)\ns_sub_u32 %[ret], s96, %[t0]\ns_cmp_lt_u32 %[ret], %[pace]\ns_cbranch_scc1 .Lt_%=\n" \
    "buffer_load_dwordx4 v[56:59], %[ob], %[rs], %[so0] offen offset:0 sc1\nbuffer_load_dwordx4 v[60:63], %[ob], %[rs], %[so0] offen offset:1024 sc1\n" \
    "buffer_load_dwordx4 v[64:67], %[ob], %[rs], %[so0] offen offset:2048 sc1\nbuffer_load_dwordx4 v[68:71], %[ob], %[rs], %[so0] offen offset:3072 sc1\n" \
    "buffer_load_dwordx4 v[72:75], %[ob], %[rs], %[so1] offen offset:0 sc1\nbuffer_load_dwordx4 v[76:79], %[ob], %[rs], %[so1] offen offset:1024 sc1\n" \
    "buffer_load_dwordx4 v[80:83], %[ob], %[rs], %[so1] offen offset:2048 sc1\nbuffer_load_dwordx4 v[84:87], %[ob], %[rs], %[so1] offen offset:3072 sc1\n" \
    "s_waitcnt lgkmcnt(0)\nv_mfma_f32_16x16x32_bf16 v[36:39], v[96:99], v[88:91], v[36:39]\n" \
    "v_mfma_f32_16x16x32_bf16 v[40:43], v[100:103], v[88:91], v[40:43]\nv_mfma_f32_16x16x32_bf16 v[44:47], v[104:107], v[88:91], v[44:47]\n" \
    "v_mfma_f32_16x16x32_bf16 v[48:51], v[108:111], v[88:91], v[48:51]\nv_mfma_f32_16x16x32_bf16 v[52:55], v[112:115], v[88:91], v[52:55]\n" \
    "buffer_load_dwordx2 v[88:89], %[ob2], %[rs], %[so0] offen offset:0 sc1\nbuffer_load_dwordx2 v[90:91], %[ob2], %[rs], %[so0] offen offset:128 sc1\n" \
    "v_mfma_f32_16x16x32_bf16 v[36:39], v[116:119], v[92:95], v[36:39]\nv_mfma_f32_16x16x32_bf16 v[40:43], v[120:123], v[92:95], v[40:43]\n" \
    "v_mfma_f32_16x16x32_bf16 v[44:47], v[124:127], v[92:95], v[44:47]\nv_mfma_f32_16x16x32_bf16 v[48:51], v[128:131], v[92:95], v[48:51]\n" \
    "v_mfma_f32_16x16x32_bf16 v[52:55], v[132:135], v[92:95], v[52:55]\nbuffer_load_dwordx2 v[92:93], %[ob2], %[rs], %[so0] offen offset:1024 sc1\n" \
    "buffer_load_dwordx2 v[94:95], %[ob2], %[rs], %[so0] offen offset:1152 sc1\nds_read_b64_tr_b16 v[96:97], %[wb1] offset:0\n" \
    "ds_read_b64_tr_b16 v[98:99], %[wb2] offset:0\nds_read_b64_tr_b16 v[100:101], %[wb1] offset:32\nds_read_b64_tr_b16 v[102:103], %[wb2] offset:32\n" \
    "ds_read_b64_tr_b16 v[104:105], %[wb1] offset:64\nds_read_b64_tr_b16 v[106:107], %[wb2] offset:64\nv_mov_b32 v35, 1\ns_nop 2\n" \
    "ds_write_b128 %[sa0], v[36:39] offset:0\ns_nop 0\nds_write_b128 %[sa0], v[40:43] offset:64\ns_nop 0\nds_write_b128 %[sa0], v[44:47] offset:128\n" \
    "s_nop 0\nds_write_b128 %[sa0], v[48:51] offset:192\ns_nop 0\nds_write_b128 %[sa0], v[52:55] offset:256\ns_mov_b64 exec, 1\nds_add_u32 %[ep0], v35\n" \
    "s_mov_b64 exec, -1\nds_read_b64_tr_b16 v[108:109], %[wb1] offset:96\nds_read_b64_tr_b16 v[110:111], %[wb2] offset:96\n" \
    "ds_read_b64_tr_b16 v[112:113], %[wb1] offset:128\nds_read_b64_tr_b16 v[114:115], %[wb2] offset:128\n" \
    "ds_read_b64_tr_b16 v[116:117], %[wb1] offset:5120\nds_read_b64_tr_b16 v[118:119], %[wb2] offset:5120\n" \
    "ds_read_b64_tr_b16 v[120:121], %[wb1] offset:5152\nds_read_b64_tr_b16 v[122:123], %[wb2] offset:5152\n" \
    "ds_read_b64_tr_b16 v[124:125], %[wb1] offset:5184\nds_read_b64_tr_b16 v[126:127], %[wb2] offset:5184\n" \
    "ds_read_b64_tr_b16 v[128:129], %[wb1] offset:5216\nds_read_b64_tr_b16 v[130:131], %[wb2] offset:5216\n" \
    "ds_read_b64_tr_b16 v[132:133], %[wb1] offset:5248\nds_read_b64_tr_b16 v[134:135], %[wb2] offset:5248\ns_waitcnt vmcnt(11)\n" \
    "v_max3_u32 v35, v56, v57, v58\nv_max_u32 v35, v35, v59\nv_cmp_eq_u32 vcc, -1, v35\ns_cbranch_vccnz .Lr0_%=\n.Lf0_%=:\n" \
    "s_waitcnt lgkmcnt(0)\nv_mfma_f32_16x16x32_bf16 v[36:39], v[96:99], v[56:59], 0\nds_read_b64_tr_b16 v[96:97], %[wb1] offset:40960\n" \
    "ds_read_b64_tr_b16 v[98:99], %[wb2] offset:40960\ns_waitcnt vmcnt(10)\nv_max3_u32 v35, v60, v61, v62\n" \
    "v_mfma_f32_16x16x32_bf16 v[40:43], v[100:103], v[56:59], 0\nds_read_b64_tr_b16 v[100:101], %[wb1] offset:40992\n" \
    "ds_read_b64_tr_b16 v[102:103], %[wb2] offset:40992\nv_max_u32 v35, v35, v63\nv_mfma_f32_16x16x32_bf16 v[44:47], v[104:107], v[56:59], 0\n" \
    "ds_read_b64_tr_b16 v[104:105], %[wb1] offset:41024\nds_read_b64_tr_b16 v[106:107], %[wb2] offset:41024\nv_cmp_eq_u32 vcc, -1, v35\n" \
    "v_mfma_f32_16x16x32_bf16 v[48:51], v[108:111], v[56:59], 0\nds_read_b64_tr_b16 v[108:109], %[wb1] offset:41056\n" \
    "ds_read_b64_tr_b16 v[110:111], %[wb2] offset:41056\nv_mfma_f32_16x16x32_bf16 v[52:55], v[112:115], v[56:59], 0\n" \
    "ds_read_b64_tr_b16 v[112:113], %[wb1] offset:41088\nds_read_b64_tr_b16 v[114:115], %[wb2] offset:41088\ns_cbranch_vccnz .Lr1_%=\n.Lf1_%=:\n" \
    "v_mfma_f32_16x16x32_bf16 v[36:39], v[116:119], v[60:63], v[36:39]\nds_read_b64_tr_b16 v[116:117], %[wb1] offset:46080\n" \
    "ds_read_b64_tr_b16 v[118:119], %[wb2] offset:46080\ns_waitcnt vmcnt(9)\nv_max3_u32 v35, v64, v65, v66\n" \
    "v_mfma_f32_16x16x32_bf16 v[40:43], v[120:123], v[60:63], v[40:43]\nds_read_b64_tr_b16 v[120:121], %[wb1] offset:46112\n" \
    "ds_read_b64_tr_b16 v[122:123], %[wb2] offset:46112\nv_max_u32 v35, v35, v67\nv_mfma_f32_16x16x32_bf16 v[44:47], v[124:127], v[60:63], v[44:47]\n" \
    "ds_read_b64_tr_b16 v[124:125], %[wb1] offset:46144\nds_read_b64_tr_b16 v[126:127], %[wb2] offset:46144\nv_cmp_eq_u32 vcc, -1, v35\n" \
    "v_mfma_f32_16x16x32_bf16 v[48:51], v[128:131], v[60:63], v[48:51]\nds_read_b64_tr_b16 v[128:129], %[wb1] offset:46176\n" \
    "ds_read_b64_tr_b16 v[130:131], %[wb2] offset:46176\nv_mfma_f32_16x16x32_bf16 v[52:55], v[132:135], v[60:63], v[52:55]\n" \
    "ds_read_b64_tr_b16 v[132:133], %[wb1] offset:46208\nds_read_b64_tr_b16 v[134:135], %[wb2] offset:46208\ns_cbranch_vccnz .Lr2_%=\n.Lf2_%=:\n" \
    "v_mfma_f32_16x16x32_bf16 v[36:39], v[136:139], v[64:67], v[36:39]\ns_waitcnt vmcnt(8)\nv_max3_u32 v35, v68, v69, v70\n" \
    "v_mfma_f32_16x16x32_bf16 v[40:43], v[140:143], v[64:67], v[40:43]\nv_max_u32 v35, v35, v71\n" \
    "v_mfma_f32_16x16x32_bf16 v[44:47], v[144:147], v[64:67], v[44:47]\nv_cmp_eq_u32 vcc, -1, v35\n" \
    "v_mfma_f32_16x16x32_bf16 v[48:51], v[148:151], v[64:67], v[48:51]\nv_mfma_f32_16x16x32_bf16 v[52:55], v[152:155], v[64:67], v[52:55]\n" \
    "s_cbranch_vccnz .Lr3_%=\n.Lf3_%=:\nv_mfma_f32_16x16x32_bf16 v[36:39], v[156:159], v[68:71], v[36:39]\ns_waitcnt vmcnt(7)\n" \
    "v_max3_u32 v35, v72, v73, v74\nv_mfma_f32_16x16x32_bf16 v[40:43], v[160:163], v[68:71], v[40:43]\nv_max_u32 v35, v35, v75\n" \
    "v_mfma_f32_16x16x32_bf16 v[44:47], v[164:167], v[68:71], v[44:47]\nv_cmp_eq_u32 vcc, -1, v35\n" \
    "v_mfma_f32_16x16x32_bf16 v[48:51], v[168:171], v[68:71], v[48:51]\nv_mfma_f32_16x16x32_bf16 v[52:55], v[172:175], v[68:71], v[52:55]\n" \
    "s_cbranch_vccnz .Lr4_%=\n.Lf4_%=:\n" \
    "v_mfma_f32_16x16x32_bf16 v[36:39], v[176:179], v[72:75], v[36:39]\ns_waitcnt vmcnt(6)\nv_max3_u32 v35, v76, v77, v78\n" \
    "v_mfma_f32_16x16x32_bf16 v[40:43], v[180:183], v[72:75], v[40:43]\nv_max_u32 v35, v35, v79\n" \
    "v_mfma_f32_16x16x32_bf16 v[44:47], v[184:187], v[72:75], v[44:47]\nv_cmp_eq_u32 vcc, -1, v35\n" \
    "v_mfma_f32_16x16x32_bf16 v[48:51], v[188:191], v[72:75], v[48:51]\nv_mfma_f32_16x16x32_bf16 v[52:55], v[192:195], v[72:75], v[52:55]\n" \
    "s_cbranch_vccnz .Lr5_%=\n.Lf5_%=:\nv_mfma_f32_16x16x32_bf16 v[36:39], v[196:199], v[76:79], v[36:39]\ns_waitcnt vmcnt(5)\n" \
    "v_max3_u32 v35, v80, v81, v82\nv_mfma_f32_16x16x32_bf16 v[40:43], v[200:203], v[76:79], v[40:43]\nv_max_u32 v35, v35, v83\n" \
    "v_mfma_f32_16x16x32_bf16 v[44:47], v[204:207], v[76:79], v[44:47]\nv_cmp_eq_u32 vcc, -1, v35\n" \
    "v_mfma_f32_16x16x32_bf16 v[48:51], v[208:211], v[76:79], v[48:51]\nv_mfma_f32_16x16x32_bf16 v[52:55], v[212:215], v[76:79], v[52:55]\n" \
    "s_cbranch_vccnz .Lr6_%=\n.Lf6_%=:\nv_mfma_f32_16x16x32_bf16 v[36:39], v[216:219], v[80:83], v[36:39]\ns_waitcnt vmcnt(4)\n" \
    "v_max3_u32 v35, v84, v85, v86\nv_mfma_f32_16x16x32_bf16 v[40:43], v[220:223], v[80:83], v[40:43]\nv_max_u32 v35, v35, v87\n" \
    "v_mfma_f32_16x16x32_bf16 v[44:47], v[224:227], v[80:83], v[44:47]\nv_cmp_eq_u32 vcc, -1, v35\n" \
    "v_mfma_f32_16x16x32_bf16 v[48:51], v[228:231], v[80:83], v[48:51]\nv_mfma_f32_16x16x32_bf16 v[52:55], v[232:235], v[80:83], v[52:55]\n" \
    "s_cbranch_vccnz .Lr7_%=\n.Lf7_%=:\nv_mfma_f32_16x16x32_bf16 v[36:39], v[236:239], v[84:87], v[36:39]\ns_waitcnt vmcnt(2)\n" \
    "v_max3_u32 v35, v88, v89, v90\nv_mfma_f32_16x16x32_bf16 v[40:43], v[240:243], v[84:87], v[40:43]\nv_max_u32 v35, v35, v91\n" \
    "v_mfma_f32_16x16x32_bf16 v[44:47], v[244:247], v[84:87], v[44:47]\nv_cmp_eq_u32 vcc, -1, v35\n" \
    "v_mfma_f32_16x16x32_bf16 v[48:51], v[248:251], v[84:87], v[48:51]\nv_mfma_f32_16x16x32_bf16 v[52:55], v[252:255], v[84:87], v[52:55]\n" \
    "s_cbranch_vccnz .Lr8_%=\n.Lf8_%=:\ns_waitcnt lgkmcnt(0)\nv_mfma_f32_16x16x32_bf16 v[36:39], v[96:99], v[88:91], v[36:39]\ns_waitcnt vmcnt(0)\n" \
    "v_max3_u32 v35, v92, v93, v94\nv_mfma_f32_16x16x32_bf16 v[40:43], v[100:103], v[88:91], v[40:43]\nv_max_u32 v35, v35, v95\n" \
    "v_mfma_f32_16x16x32_bf16 v[44:47], v[104:107], v[88:91], v[44:47]\nv_cmp_eq_u32 vcc, -1, v35\n" \
    "v_mfma_f32_16x16x32_bf16 v[48:51], v[108:111], v[88:91], v[48:51]\nv_mfma_f32_16x16x32_bf16 v[52:55], v[112:115], v[88:91], v[52:55]\n" \
    "s_cbranch_vccnz .Lr9_%=\n.Lf9_%=:\n" \
    "v_mfma_f32_16x16x32_bf16 v[36:39], v[116:119], v[92:95], v[36:39]\nv_mfma_f32_16x16x32_bf16 v[40:43], v[120:123], v[92:95], v[40:43]\n" \
    "v_mfma_f32_16x16x32_bf16 v[44:47], v[124:127], v[92:95], v[44:47]\nv_mfma_f32_16x16x32_bf16 v[48:51], v[128:131], v[92:95], v[48:51]\n" \
    "v_mfma_f32_16x16x32_bf16 v[52:55], v[132:135], v[92:95], v[52:55]\ns_branch .Ldone_%=\n.Lr0_%=:\ns_add_i32 %[cap], %[cap], -1\n" \
    "s_cmp_le_i32 %[cap], 0\ns_cbranch_scc1 .Lf0_%=\ns_mov_b32 %[ret], 0\ns_waitcnt vmcnt(0)\ns_branch .Ll0_%=\n.Lr1_%=:\ns_add_i32 %[cap], %[cap], -1\n" \
    "s_cmp_le_i32 %[cap], 0\ns_cbranch_scc1 .Lf1_%=\ns_mov_b32 %[ret], 1\ns_waitcnt vmcnt(0)\ns_branch .Ll1_%=\n.Lr2_%=:\ns_add_i32 %[cap], %[cap], -1\n" \
    "s_cmp_le_i32 %[cap], 0\ns_cbranch_scc1 .Lf2_%=\ns_mov_b32 %[ret], 2\ns_waitcnt vmcnt(0)\ns_branch .Ll2_%=\n.Lr3_%=:\ns_add_i32 %[cap], %[cap], -1\n" \
    "s_cmp_le_i32 %[cap], 0\ns_cbranch_scc1 .Lf3_%=\ns_mov_b32 %[ret], 3\ns_waitcnt vmcnt(0)\ns_branch .Ll3_%=\n.Lr4_%=:\ns_add_i32 %[cap], %[cap], -1\n" \
    "s_cmp_le_i32 %[cap], 0\ns_cbranch_scc1 .Lf4_%=\ns_mov_b32 %[ret], 4\ns_waitcnt vmcnt(0)\ns_branch .Ll4_%=\n.Lr5_%=:\ns_add_i32 %[cap], %[cap], -1\n" \
    "s_cmp_le_i32 %[cap], 0\ns_cbranch_scc1 .Lf5_%=\ns_mov_b32 %[ret], 5\ns_waitcnt vmcnt(0)\ns_branch .Ll5_%=\n.Lr6_%=:\ns_add_i32 %[cap], %[cap], -1\n" \
    "s_cmp_le_i32 %[cap], 0\ns_cbranch_scc1 .Lf6_%=\ns_mov_b32 %[ret], 6\ns_waitcnt vmcnt(0)\ns_branch .Ll6_%=\n.Lr7_%=:\ns_add_i32 %[cap], %[cap], -1\n" \
    "s_cmp_le_i32 %[cap], 0\ns_cbranch_scc1 .Lf7_%=\ns_mov_b32 %[ret], 7\ns_waitcnt vmcnt(0)\ns_branch .Ll7_%=\n.Lr8_%=:\ns_add_i32 %[cap], %[cap], -1\n" \
    "s_cmp_le_i32 %[cap], 0\ns_cbranch_scc1 .Lf8_%=\ns_mov_b32 %[ret], 8\ns_waitcnt vmcnt(0)\ns_branch .Ll8_%=\n.Lr9_%=:\ns_add_i32 %[cap], %[cap], -1\n" \
    "s_cmp_le_i32 %[cap], 0\ns_cbranch_scc1 .Lf9_%=\ns_mov_b32 %[ret], 9\ns_waitcnt vmcnt(0)\ns_branch .Ll9_%=\n.Ll0_%=:\n" \
    "buffer_load_dwordx4 v[56:59], %[ob], %[rs], %[so0] offen offset:0 sc1\n.Ll1_%=:\n" \
    "buffer_load_dwordx4 v[60:63], %[ob], %[rs], %[so0] offen offset:1024 sc1\n.Ll2_%=:\n" \
    "buffer_load_dwordx4 v[64:67], %[ob], %[rs], %[so0] offen offset:2048 sc1\n.Ll3_%=:\n" \
    "buffer_load_dwordx4 v[68:71], %[ob], %[rs], %[so0] offen offset:3072 sc1\n.Ll4_%=:\n" \
    "buffer_load_dwordx4 v[72:75], %[ob], %[rs], %[so1] offen offset:0 sc1\n.Ll5_%=:\n" \
    "buffer_load_dwordx4 v[76:79], %[ob], %[rs], %[so1] offen offset:1024 sc1\n.Ll6_%=:\n" \
    "buffer_load_dwordx4 v[80:83], %[ob], %[rs], %[so1] offen offset:2048 sc1\n.Ll7_%=:\n" \
    "buffer_load_dwordx4 v[84:87], %[ob], %[rs], %[so1] offen offset:3072 sc1\n.Ll8_%=:\n" \
    "buffer_load_dwordx2 v[88:89], %[ob2], %[rs], %[so0] offen offset:0 sc1\nbuffer_load_dwordx2 v[90:91], %[ob2], %[rs], %[so0] offen offset:128 sc1\n" \
    ".Ll9_%=:\nbuffer_load_dwordx2 v[92:93], %[ob2], %[rs], %[so0] offen offset:1024 sc1\n" \
    "buffer_load_dwordx2 v[94:95], %[ob2], %[rs], %[so0] offen offset:1152 sc1\ns_cmp_eq_u32 %[ret], 0\ns_cbranch_scc1 .Lc0_%=\ns_cmp_eq_u32 %[ret], 1\n" \
    "s_cbranch_scc1 .Lc1_%=\ns_cmp_eq_u32 %[ret], 2\ns_cbranch_scc1 .Lc2_%=\ns_cmp_eq_u32 %[ret], 3\ns_cbranch_scc1 .Lc3_%=\ns_cmp_eq_u32 %[ret], 4\n" \
    "s_cbranch_scc1 .Lc4_%=\ns_cmp_eq_u32 %[ret], 5\ns_cbranch_scc1 .Lc5_%=\ns_cmp_eq_u32 %[ret], 6\ns_cbranch_scc1 .Lc6_%=\ns_cmp_eq_u32 %[ret], 7\n" \
    "s_cbranch_scc1 .Lc7_%=\ns_cmp_eq_u32 %[ret], 8\ns_cbranch_scc1 .Lc8_%=\ns_cmp_eq_u32 %[ret], 9\ns_cbranch_scc1 .Lc9_%=\n.Lc0_%=:\n" \
    "s_waitcnt vmcnt(11)\nv_max3_u32 v35, v56, v57, v58\nv_max_u32 v35, v35, v59\nv_cmp_eq_u32 vcc, -1, v35\ns_cbranch_vccnz .Lr0_%=\ns_branch .Lf0_%=\n" \
    ".Lc1_%=:\ns_waitcnt vmcnt(10)\nv_max3_u32 v35, v60, v61, v62\nv_max_u32 v35, v35, v63\nv_cmp_eq_u32 vcc, -1, v35\ns_cbranch_vccnz .Lr1_%=\n" \
    "s_branch .Lf1_%=\n.Lc2_%=:\ns_waitcnt vmcnt(9)\nv_max3_u32 v35, v64, v65, v66\nv_max_u32 v35, v35, v67\nv_cmp_eq_u32 vcc, -1, v35\n" \
    "s_cbranch_vccnz .Lr2_%=\ns_branch .Lf2_%=\n.Lc3_%=:\ns_waitcnt vmcnt(8)\nv_max3_u32 v35, v68, v69, v70\nv_max_u32 v35, v35, v71\n" \
    "v_cmp_eq_u32 vcc, -1, v35\ns_cbranch_vccnz .Lr3_%=\ns_branch .Lf3_%=\n.Lc4_%=:\ns_waitcnt vmcnt(7)\nv_max3_u32 v35, v72, v73, v74\n" \
    "v_max_u32 v35, v35, v75\nv_cmp_eq_u32 vcc, -1, v35\ns_cbranch_vccnz .Lr4_%=\ns_branch .Lf4_%=\n.Lc5_%=:\ns_waitcnt vmcnt(6)\n" \
    "v_max3_u32 v35, v76, v77, v78\nv_max_u32 v35, v35, v79\nv_cmp_eq_u32 vcc, -1, v35\ns_cbranch_vccnz .Lr5_%=\ns_branch .Lf5_%=\n.Lc6_%=:\n" \
    "s_waitcnt vmcnt(5)\nv_max3_u32 v35, v80, v81, v82\nv_max_u32 v35, v35, v83\nv_cmp_eq_u32 vcc, -1, v35\ns_cbranch_vccnz .Lr6_%=\ns_branch .Lf6_%=\n" \
    ".Lc7_%=:\ns_waitcnt vmcnt(4)\nv_max3_u32 v35, v84, v85, v86\nv_max_u32 v35, v35, v87\nv_cmp_eq_u32 vcc, -1, v35\ns_cbranch_vccnz .Lr7_%=\n" \
    "s_branch .Lf7_%=\n.Lc8_%=:\ns_waitcnt vmcnt(2)\nv_max3_u32 v35, v88, v89, v90\nv_max_u32 v35, v35, v91\nv_cmp_eq_u32 vcc, -1, v35\n" \
    "s_cbranch_vccnz .Lr8_%=\ns_branch .Lf8_%=\n.Lc9_%=:\ns_waitcnt vmcnt(0)\nv_max3_u32 v35, v92, v93, v94\nv_max_u32 v35, v35, v95\n" \
    "v_cmp_eq_u32 vcc, -1, v35\ns_cbranch_vccnz .Lr9_%=\ns_branch .Lf9_%=\n.Ldone_%=:\nv_mov_b32 v35, 1\ns_nop 2\nds_write_b128 %[sa], v[36:39] offset:0\n" \
    "s_nop 0\nds_write_b128 %[sa], v[40:43] offset:64\ns_nop 0\nds_write_b128 %[sa], v[44:47] offset:128\ns_nop 0\n" \
    "ds_write_b128 %[sa], v[48:51] offset:192\ns_nop 0\nds_write_b128 %[sa], v[52:55] offset:256\ns_mov_b64 exec, 1\nds_add_u32 %[ep], v35\n" \
    "s_mov_b64 exec, -1\n.Lx_%=:\ns_nop 7\n"
#define CLOB_PROJ2 "v34", "v35", "v36", "v37", "v38", "v39", "v40", "v41", "v42", "v43", "v44", "v45", "v46", "v47", "v48", "v49", "v50", "v51", "v52", "v53", "v54", "v55"
#define CLOB_GEMM2 "v35", "v36", "v37", "v38", "v39", "v40", "v41", "v42", "v43", "v44", "v45", "v46", "v47", "v48", "v49", "v50", "v51", "v52", "v53", "v54", "v55", "v56", "v57", "v58", "v59", "v60", "v61", "v62", "v63", "v64", "v65", "v66", "v67", "v68", "v69", "v70", "v71", "v72", "v73", "v74", "v75", "v76", "v77", "v78", "v79", "v80", "v81", "v82", "v83", "v84", "v85", "v86", "v87", "v88", "v89", "v90", "v91", "v92", "v93", "v94", "v95", "v96", "v97", "v98", "v99", "v100", "v101", "v102", "v103", "v104", "v105", "v106", "v107", "v108", "v109", "v110", "v111", "v112", "v113", "v114", "v115", "v116", "v117", "v118", "v119", "v120", "v121", "v122", "v123", "v124", "v125", "v126", "v127"
#define ASM_ATTN_A \
    ".p2align 3\nv_mov_b32 v43, 0xdddddddd\nv_mov_b32 v42, 1\nv_mov_b32 v56, 0\nv_mov_b32 v72, 0\nv_mov_b32 v57, 0\nv_mov_b32 v73, 0\nv_mov_b32 v58, 0\n" \
    "v_mov_b32 v74, 0\nv_mov_b32 v59, 0\nv_mov_b32 v75, 0\nv_mov_b32 v60, 0\nv_mov_b32 v76, 0\nv_mov_b32 v61, 0\nv_mov_b32 v77, 0\nv_mov_b32 v62, 0\n" \
    "v_mov_b32 v78, 0\nv_mov_b32 v63, 0\nv_mov_b32 v79, 0\nv_mov_b32 v64, 0\nv_mov_b32 v80, 0\nv_mov_b32 v65, 0\nv_mov_b32 v81, 0\nv_mov_b32 v66, 0\n" \
    "v_mov_b32 v82, 0\nv_mov_b32 v67, 0\nv_mov_b32 v83, 0\n.Lq_%=:\ns_nop 0\nds_read_b32 v41, %[qd]\nds_read_b128 v[44:47], v6 offset:0\n" \
    "ds_read_b64 v[48:49], v10 offset:0\nds_read_b128 v[50:53], v6 offset:1280\nds_read_b64 v[54:55], v10 offset:1280\ns_waitcnt lgkmcnt(0)\n" \
    "v_readfirstlane_b32 %[st], v41\ns_add_i32 %[cap], %[cap], -1\ns_cmp_le_i32 %[cap], 0\ns_cbranch_scc1 .Lq2_%=\ns_cmp_ge_u32 %[st], 4\n" \
    "s_cbranch_scc0 .Lq_%=\n.Lq2_%=:\ns_waitcnt vmcnt(16)\nv_mfma_f32_32x32x4_2b_bf16 v[88:119], v[248:249], v[48:49], 0\ns_nop 15\ns_nop 1\n" \
    "s_waitcnt vmcnt(15)\ns_nop 0\nv_mfma_f32_32x32x16_bf16 v[88:103], v[152:155], v[44:47], v[88:103]\ns_waitcnt vmcnt(14)\ns_nop 0\n" \
    "v_mfma_f32_32x32x16_bf16 v[104:119], v[156:159], v[44:47], v[104:119]\ns_nop 10\ns_nop 0\n" \
    "v_exp_f32 v88, v88\nv_exp_f32 v89, v89\nv_exp_f32 v90, v90\nv_exp_f32 v91, v91\nv_exp_f32 v92, v92\nv_exp_f32 v93, v93\n" \
    "v_mfma_f32_32x32x4_2b_bf16 v[120:151], v[248:249], v[54:55], 0\nv_exp_f32 v94, v94\nv_exp_f32 v95, v95\nv_exp_f32 v96, v96\nv_exp_f32 v97, v97\n" \
    "v_exp_f32 v98, v98\nv_exp_f32 v99, v99\nv_exp_f32 v100, v100\nv_exp_f32 v101, v101\ns_waitcnt vmcnt(10)\ns_nop 0\n" \
    "v_smfmac_f32_32x32x32_bf16 v[56:71], v[184:187], v[88:95], v43\ns_add_i32 %[st], %[rot], 3\ns_and_b32 %[st], %[st], 3\ns_mulk_i32 %[st], 5120\n" \
    "s_add_i32 %[st], %[st], %[sb]\nbuffer_load_dwordx2 v[254:255], %[k2o], %[rs], %[st] offen sc1\n" \
    "buffer_load_dwordx4 v[176:179], %[ko], %[rs], %[st] offen offset:0 sc1\nbuffer_load_dwordx4 v[180:183], %[ko], %[rs], %[st] offen offset:1024 sc1\n" \
    "s_mov_b64 exec, -1\nv_exp_f32 v102, v102\nv_exp_f32 v103, v103\nv_exp_f32 v104, v104\nv_exp_f32 v105, v105\ns_waitcnt vmcnt(12)\n" \
    "v_smfmac_f32_32x32x32_bf16 v[56:71], v[188:191], v[96:103], v43\nv_exp_f32 v106, v106\nv_exp_f32 v107, v107\nv_exp_f32 v108, v108\n" \
    "v_exp_f32 v109, v109\nv_mfma_f32_32x32x16_bf16 v[120:135], v[152:155], v[50:53], v[120:135]\nv_exp_f32 v110, v110\nv_exp_f32 v111, v111\n" \
    "v_exp_f32 v112, v112\nv_exp_f32 v113, v113\nv_mfma_f32_32x32x16_bf16 v[136:151], v[156:159], v[50:53], v[136:151]\nv_exp_f32 v114, v114\n" \
    "v_exp_f32 v115, v115\nv_exp_f32 v116, v116\nv_exp_f32 v117, v117\ns_waitcnt vmcnt(11)\ns_nop 0\n" \
    "v_smfmac_f32_32x32x32_bf16 v[56:71], v[192:195], v[104:111], v43\ns_add_i32 %[st], %[rot], 2\ns_and_b32 %[st], %[st], 3\ns_mulk_i32 %[st], 5120\n" \
    "s_add_i32 %[st], %[st], %[sb]\ns_mov_b32 exec_lo, 0xfffff\ns_mov_b32 exec_hi, 0xfffff\n" \
    "buffer_load_dwordx4 v[216:219], %[vo], %[rs], %[st] offen offset:0 sc1\nbuffer_load_dwordx4 v[220:223], %[vo], %[rs], %[st] offen offset:640 sc1\n" \
    "buffer_load_dwordx4 v[224:227], %[vo], %[rs], %[st] offen offset:1280 sc1\nbuffer_load_dwordx4 v[228:231], %[vo], %[rs], %[st] offen offset:1920 sc1\n" \
    "s_mov_b64 exec, -1\nv_exp_f32 v118, v118\nv_exp_f32 v119, v119\nv_exp_f32 v120, v120\nv_exp_f32 v121, v121\ns_waitcnt vmcnt(14)\n" \
    "v_smfmac_f32_32x32x32_bf16 v[56:71], v[196:199], v[112:119], v43\nv_exp_f32 v122, v122\nv_exp_f32 v123, v123\nv_exp_f32 v124, v124\n" \
    "v_exp_f32 v125, v125\nv_mfma_f32_32x32x4_2b_bf16 v[88:119], v[250:251], v[48:49], 0\nv_exp_f32 v126, v126\nv_exp_f32 v127, v127\n" \
    "v_exp_f32 v128, v128\nv_exp_f32 v129, v129\nv_exp_f32 v130, v130\nv_exp_f32 v131, v131\nv_exp_f32 v132, v132\nv_exp_f32 v133, v133\n" \
    "v_smfmac_f32_32x32x32_bf16 v[72:87], v[184:187], v[120:127], v43\ns_add_i32 %[st], %[rot], 3\ns_and_b32 %[st], %[st], 3\ns_mulk_i32 %[st], 5120\n" \
    "s_add_i32 %[st], %[st], %[sb]\ns_mov_b32 exec_lo, 0xfffff\ns_mov_b32 exec_hi, 0xfffff\n" \
    "buffer_load_dwordx4 v[232:235], %[vo], %[rs], %[st] offen offset:0 sc1\nbuffer_load_dwordx4 v[236:239], %[vo], %[rs], %[st] offen offset:640 sc1\n" \
    "buffer_load_dwordx4 v[240:243], %[vo], %[rs], %[st] offen offset:1280 sc1\nbuffer_load_dwordx4 v[244:247], %[vo], %[rs], %[st] offen offset:1920 sc1\n" \
    "s_mov_b64 exec, 1\ns_nop 0\nds_add_u32 %[qd], v42 offset:8\ns_mov_b64 exec, -1\nv_exp_f32 v134, v134\nv_exp_f32 v135, v135\nv_exp_f32 v136, v136\n" \
    "v_exp_f32_e64 v137, v137\nv_smfmac_f32_32x32x32_bf16 v[72:87], v[188:191], v[128:135], v43\nv_exp_f32 v138, v138\nv_exp_f32 v139, v139\n" \
    "v_exp_f32 v140, v140\nv_exp_f32 v141, v141\nv_mfma_f32_32x32x16_bf16 v[88:103], v[160:163], v[44:47], v[88:103]\nv_exp_f32 v142, v142\n" \
    "v_exp_f32 v143, v143\nv_exp_f32 v144, v144\nv_exp_f32 v145, v145\nv_mfma_f32_32x32x16_bf16 v[104:119], v[164:167], v[44:47], v[104:119]\n" \
    "v_exp_f32 v146, v146\nv_exp_f32 v147, v147\nv_exp_f32 v148, v148\nv_exp_f32 v149, v149\n" \
    "v_smfmac_f32_32x32x32_bf16 v[72:87], v[192:195], v[136:143], v43\nv_exp_f32 v150, v150\nv_exp_f32 v151, v151\nv_exp_f32 v88, v88\nv_exp_f32 v89, v89\n" \
    "v_smfmac_f32_32x32x32_bf16 v[72:87], v[196:199], v[144:151], v43\nv_exp_f32 v90, v90\nv_exp_f32 v91, v91\nv_exp_f32 v92, v92\nv_exp_f32 v93, v93\n" \
    "s_waitcnt vmcnt(17)\ns_nop 0\nv_mfma_f32_32x32x4_2b_bf16 v[120:151], v[252:253], v[48:49], 0\nv_exp_f32 v94, v94\nv_exp_f32 v95, v95\n" \
    "v_exp_f32 v96, v96\nv_exp_f32 v97, v97\nv_exp_f32 v98, v98\nv_exp_f32 v99, v99\nv_exp_f32 v100, v100\nv_exp_f32 v101, v101\ns_waitcnt vmcnt(14)\n" \
    "s_nop 0\nv_smfmac_f32_32x32x32_bf16 v[56:71], v[200:203], v[88:95], v43\nv_exp_f32 v102, v102\nv_exp_f32 v103, v103\nv_exp_f32 v104, v104\n" \
    "v_exp_f32 v105, v105\ns_waitcnt vmcnt(13)\ns_nop 0\nv_smfmac_f32_32x32x32_bf16 v[56:71], v[204:207], v[96:103], v43\nv_exp_f32 v106, v106\n" \
    "v_exp_f32 v107, v107\nv_exp_f32 v108, v108\nv_exp_f32 v109, v109\nv_mfma_f32_32x32x16_bf16 v[120:135], v[168:171], v[44:47], v[120:135]\n" \
    "v_exp_f32 v110, v110\nv_exp_f32 v111, v111\nv_exp_f32 v112, v112\nv_exp_f32 v113, v113\n" \
    "v_mfma_f32_32x32x16_bf16 v[136:151], v[172:175], v[44:47], v[136:151]\nv_exp_f32 v114, v114\nv_exp_f32 v115, v115\nv_exp_f32 v116, v116\n" \
    "v_exp_f32 v117, v117\ns_waitcnt vmcnt(12)\ns_nop 0\nv_smfmac_f32_32x32x32_bf16 v[56:71], v[208:211], v[104:111], v43\nv_exp_f32 v118, v118\n" \
    "v_exp_f32 v119, v119\nv_exp_f32 v120, v120\nv_exp_f32 v121, v121\ns_waitcnt vmcnt(11)\ns_nop 0\n" \
    "v_smfmac_f32_32x32x32_bf16 v[56:71], v[212:215], v[112:119], v43\ns_waitcnt lgkmcnt(0)\nv_exp_f32 v122, v122\nv_exp_f32 v123, v123\n" \
    "v_exp_f32 v124, v124\nv_exp_f32 v125, v125\ns_waitcnt vmcnt(10)\nv_mfma_f32_32x32x4_2b_bf16 v[88:119], v[254:255], v[48:49], 0\nv_exp_f32 v126, v126\n" \
    "v_exp_f32 v127, v127\nv_exp_f32 v128, v128\nv_exp_f32 v129, v129\nv_exp_f32 v130, v130\nv_exp_f32 v131, v131\nv_exp_f32 v132, v132\n" \
    "v_exp_f32 v133, v133\ns_waitcnt vmcnt(7)\ns_nop 0\nv_smfmac_f32_32x32x32_bf16 v[56:71], v[216:219], v[120:127], v43\nv_exp_f32 v134, v134\n" \
    "v_exp_f32 v135, v135\nv_exp_f32 v136, v136\nv_exp_f32 v137, v137\ns_waitcnt vmcnt(6)\ns_nop 0\n" \
    "v_smfmac_f32_32x32x32_bf16 v[56:71], v[220:223], v[128:135], v43\nv_exp_f32 v138, v138\nv_exp_f32 v139, v139\nv_exp_f32 v140, v140\n" \
    "v_exp_f32 v141, v141\nv_mfma_f32_32x32x16_bf16 v[88:103], v[176:179], v[44:47], v[88:103]\nv_exp_f32 v142, v142\nv_exp_f32 v143, v143\n" \
    "v_exp_f32 v144, v144\nv_exp_f32 v145, v145\nv_mfma_f32_32x32x16_bf16 v[104:119], v[180:183], v[44:47], v[104:119]\nv_exp_f32 v146, v146\n" \
    "v_exp_f32 v147, v147\nv_exp_f32 v148, v148\nv_exp_f32 v149, v149\ns_waitcnt vmcnt(5)\ns_nop 0\n" \
    "v_smfmac_f32_32x32x32_bf16 v[56:71], v[224:227], v[136:143], v43\nv_exp_f32 v150, v150\nv_exp_f32 v151, v151\n" \
    "v_exp_f32 v88, v88\nv_exp_f32 v89, v89\ns_waitcnt vmcnt(4)\ns_nop 0\nv_smfmac_f32_32x32x32_bf16 v[56:71], v[228:231], v[144:151], v43\n" \
    "v_exp_f32 v90, v90\nv_exp_f32 v91, v91\nv_exp_f32 v92, v92\nv_exp_f32 v93, v93\nv_mfma_f32_32x32x4_2b_bf16 v[120:151], v[250:251], v[54:55], 0\n" \
    "v_exp_f32 v94, v94\nv_exp_f32 v95, v95\nv_exp_f32 v96, v96\nv_exp_f32 v97, v97\nv_exp_f32 v98, v98\nv_exp_f32 v99, v99\nv_exp_f32 v100, v100\n" \
    "v_exp_f32 v101, v101\ns_waitcnt vmcnt(3)\ns_nop 0\nv_smfmac_f32_32x32x32_bf16 v[56:71], v[232:235], v[88:95], v43\nv_exp_f32 v102, v102\n" \
    "v_exp_f32 v103, v103\nv_exp_f32 v104, v104\nv_exp_f32 v105, v105\ns_waitcnt vmcnt(2)\ns_nop 0\n" \
    "v_smfmac_f32_32x32x32_bf16 v[56:71], v[236:239], v[96:103], v43\nv_exp_f32 v106, v106\nv_exp_f32 v107, v107\nv_exp_f32 v108, v108\n" \
    "v_exp_f32 v109, v109\nv_mfma_f32_32x32x16_bf16 v[120:135], v[160:163], v[50:53], v[120:135]\nv_exp_f32 v110, v110\nv_exp_f32 v111, v111\n" \
    "v_exp_f32 v112, v112\nv_exp_f32 v113, v113\nv_mfma_f32_32x32x16_bf16 v[136:151], v[164:167], v[50:53], v[136:151]\nv_exp_f32 v114, v114\n" \
    "v_exp_f32 v115, v115\nv_exp_f32 v116, v116\nv_exp_f32 v117, v117\ns_waitcnt vmcnt(1)\ns_nop 0\n" \
    "v_smfmac_f32_32x32x32_bf16 v[56:71], v[240:243], v[104:111], v43\nv_exp_f32 v118, v118\nv_exp_f32 v119, v119\nv_exp_f32 v120, v120\n" \
    "v_exp_f32 v121, v121\ns_waitcnt vmcnt(0)\ns_nop 0\nv_smfmac_f32_32x32x32_bf16 v[56:71], v[244:247], v[112:119], v43\nv_exp_f32 v122, v122\n" \
    "v_exp_f32 v123, v123\nv_exp_f32 v124, v124\nv_exp_f32 v125, v125\nv_mfma_f32_32x32x4_2b_bf16 v[88:119], v[252:253], v[54:55], 0\n" \
    "v_exp_f32 v126, v126\nv_exp_f32 v127, v127\nv_exp_f32 v128, v128\nv_exp_f32 v129, v129\nv_mov_b32 v42, 1\nv_exp_f32 v130, v130\nv_exp_f32 v131, v131\n" \
    "v_exp_f32 v132, v132\nv_exp_f32_e64 v133, v133\nv_smfmac_f32_32x32x32_bf16 v[72:87], v[200:203], v[120:127], v43\n" \
    "ds_write_b128 v2, v[56:59] offset:0\nv_exp_f32 v134, v134\nv_exp_f32 v135, v135\nv_exp_f32 v136, v136\nv_exp_f32 v137, v137\n" \
    "v_smfmac_f32_32x32x32_bf16 v[72:87], v[204:207], v[128:135], v43\nds_write_b128 v2, v[60:63] offset:1024\nv_exp_f32 v138, v138\nv_exp_f32 v139, v139\n" \
    "v_exp_f32 v140, v140\nv_exp_f32 v141, v141\nv_mfma_f32_32x32x16_bf16 v[88:103], v[168:171], v[50:53], v[88:103]\n" \
    "ds_write_b128 v2, v[64:67] offset:2048\nv_exp_f32 v142, v142\nv_exp_f32 v143, v143\nv_exp_f32 v144, v144\nv_exp_f32 v145, v145\n" \
    "v_mfma_f32_32x32x16_bf16 v[104:119], v[172:175], v[50:53], v[104:119]\nds_write2_b32 %[f0], v42, v42 offset0:0 offset1:4\nv_exp_f32 v146, v146\n" \
    "v_exp_f32 v147, v147\nv_exp_f32 v148, v148\nv_exp_f32 v149, v149\nv_smfmac_f32_32x32x32_bf16 v[72:87], v[208:211], v[136:143], v43\n" \
    "v_exp_f32 v150, v150\nv_exp_f32 v151, v151\nv_exp_f32 v88, v88\nv_exp_f32 v89, v89\nv_smfmac_f32_32x32x32_bf16 v[72:87], v[212:215], v[144:151], v43\n" \
    "ds_read_b128 v[44:47], v7 offset:0\nv_exp_f32 v90, v90\nv_exp_f32 v91, v91\nv_exp_f32 v92, v92\nv_exp_f32 v93, v93\n" \
    "v_mfma_f32_32x32x4_2b_bf16 v[120:151], v[254:255], v[54:55], 0\nv_exp_f32 v94, v94\nv_exp_f32 v95, v95\nv_exp_f32 v96, v96\nv_exp_f32 v97, v97\n" \
    "v_exp_f32 v98, v98\nv_exp_f32 v99, v99\nv_exp_f32 v100, v100\nv_exp_f32 v101, v101\nv_smfmac_f32_32x32x32_bf16 v[72:87], v[216:219], v[88:95], v43\n" \
    "ds_read_b64 v[48:49], v11 offset:0\nv_exp_f32 v102, v102\nv_exp_f32 v103, v103\nv_exp_f32 v104, v104\nv_exp_f32 v105, v105\n" \
    "v_smfmac_f32_32x32x32_bf16 v[72:87], v[220:223], v[96:103], v43\nv_exp_f32 v106, v106\nv_exp_f32 v107, v107\nv_exp_f32 v108, v108\n" \
    "v_exp_f32 v109, v109\nv_mfma_f32_32x32x16_bf16 v[120:135], v[176:179], v[50:53], v[120:135]\nv_exp_f32 v110, v110\nv_exp_f32 v111, v111\n" \
    "v_exp_f32 v112, v112\nv_exp_f32 v113, v113\nv_mfma_f32_32x32x16_bf16 v[136:151], v[180:183], v[50:53], v[136:151]\nv_exp_f32 v114, v114\n" \
    "v_exp_f32 v115, v115\nv_exp_f32 v116, v116\nv_exp_f32 v117, v117\nv_smfmac_f32_32x32x32_bf16 v[72:87], v[224:227], v[104:111], v43\n" \
    "v_exp_f32 v118, v118\nv_exp_f32 v119, v119\nv_exp_f32 v120, v120\nv_exp_f32 v121, v121\n" \
    "v_smfmac_f32_32x32x32_bf16 v[72:87], v[228:231], v[112:119], v43\nds_read_b32 v41, %[f0] offset:4\ns_waitcnt lgkmcnt(1)\nv_exp_f32 v122, v122\n" \
    "v_exp_f32 v123, v123\nv_exp_f32 v124, v124\nv_exp_f32_e64 v125, v125\nv_mfma_f32_32x32x4_2b_bf16 v[88:119], v[248:249], v[48:49], 0\n" \
    "v_exp_f32 v126, v126\nv_exp_f32 v127, v127\nv_exp_f32 v128, v128\nv_exp_f32 v129, v129\nv_exp_f32 v130, v130\nv_exp_f32 v131, v131\n" \
    "v_exp_f32 v132, v132\nv_exp_f32 v133, v133\nv_smfmac_f32_32x32x32_bf16 v[72:87], v[232:235], v[120:127], v43\nds_read_b128 v[56:59], v3 offset:0\n" \
    "v_exp_f32 v134, v134\nv_exp_f32 v135, v135\nv_exp_f32 v136, v136\nv_exp_f32 v137, v137\n" \
    "v_smfmac_f32_32x32x32_bf16 v[72:87], v[236:239], v[128:135], v43\nds_read_b128 v[60:63], v3 offset:1024\ns_waitcnt lgkmcnt(2)\nv_exp_f32 v138, v138\n" \
    "v_exp_f32 v139, v139\nv_readfirstlane_b32 %[st], v41\nv_exp_f32 v140, v140\nv_exp_f32 v141, v141\n" \
    "v_mfma_f32_32x32x16_bf16 v[88:103], v[152:155], v[44:47], v[88:103]\nds_read_b128 v[64:67], v3 offset:2048\nv_exp_f32 v142, v142\n" \
    "v_exp_f32 v143, v143\nv_exp_f32 v144, v144\nv_exp_f32 v145, v145\nv_mfma_f32_32x32x16_bf16 v[104:119], v[156:159], v[44:47], v[104:119]\n" \
    "s_cmp_ge_u32 %[st], 1\ns_cbranch_scc0 .Lrt1_%=\n.Lok1_%=:\nv_exp_f32 v146, v146\nv_exp_f32 v147, v147\nv_exp_f32 v148, v148\nv_exp_f32 v149, v149\n" \
    "v_smfmac_f32_32x32x32_bf16 v[72:87], v[240:243], v[136:143], v43\nv_exp_f32 v150, v150\nv_exp_f32 v151, v151\n" \
    "v_exp_f32 v88, v88\nv_exp_f32 v89, v89\nv_smfmac_f32_32x32x32_bf16 v[72:87], v[244:247], v[144:151], v43\nv_exp_f32 v90, v90\nv_exp_f32 v91, v91\n" \
    "v_exp_f32 v92, v92\nv_exp_f32 v93, v93\nv_mfma_f32_32x32x4_2b_bf16 v[120:151], v[250:251], v[48:49], 0\ns_waitcnt lgkmcnt(0)\nv_exp_f32 v94, v94\n" \
    "v_exp_f32 v95, v95\nv_exp_f32 v96, v96\nv_exp_f32 v97, v97\nv_mov_b32 v42, 2\nv_exp_f32 v98, v98\nv_exp_f32 v99, v99\nv_exp_f32 v100, v100\n" \
    "v_exp_f32 v101, v101\nv_smfmac_f32_32x32x32_bf16 v[56:71], v[184:187], v[88:95], v43\nds_write_b128 v2, v[72:75] offset:12288\nv_exp_f32 v102, v102\n" \
    "v_exp_f32 v103, v103\nv_exp_f32 v104, v104\nv_exp_f32 v105, v105\nv_smfmac_f32_32x32x32_bf16 v[56:71], v[188:191], v[96:103], v43\n" \
    "ds_write_b128 v2, v[76:79] offset:13312\nv_exp_f32 v106, v106\nv_exp_f32 v107, v107\nv_exp_f32 v108, v108\nv_exp_f32 v109, v109\n" \
    "v_mfma_f32_32x32x16_bf16 v[120:135], v[160:163], v[44:47], v[120:135]\nds_write_b128 v2, v[80:83] offset:14336\nv_exp_f32 v110, v110\n" \
    "v_exp_f32 v111, v111\nv_exp_f32 v112, v112\nv_exp_f32 v113, v113\nv_mfma_f32_32x32x16_bf16 v[136:151], v[164:167], v[44:47], v[136:151]\n" \
    "ds_write2_b32 %[f0], v42, v42 offset0:0 offset1:4\nv_exp_f32 v114, v114\nv_exp_f32 v115, v115\nv_exp_f32 v116, v116\nv_exp_f32 v117, v117\n" \
    "v_smfmac_f32_32x32x32_bf16 v[56:71], v[192:195], v[104:111], v43\nv_exp_f32 v118, v118\nv_exp_f32 v119, v119\nv_exp_f32 v120, v120\n" \
    "v_exp_f32 v121, v121\nv_smfmac_f32_32x32x32_bf16 v[56:71], v[196:199], v[112:119], v43\nv_exp_f32 v122, v122\nv_exp_f32 v123, v123\n" \
    "v_exp_f32 v124, v124\nv_exp_f32 v125, v125\nv_mfma_f32_32x32x4_2b_bf16 v[88:119], v[252:253], v[48:49], 0\nv_exp_f32 v126, v126\n" \
    "v_exp_f32 v127, v127\nv_exp_f32 v128, v128\nv_exp_f32 v129, v129\nv_exp_f32 v130, v130\nv_exp_f32 v131, v131\nv_exp_f32 v132, v132\n" \
    "v_exp_f32 v133, v133\nv_smfmac_f32_32x32x32_bf16 v[56:71], v[200:203], v[120:127], v43\nv_exp_f32 v134, v134\nv_exp_f32 v135, v135\n" \
    "v_exp_f32 v136, v136\nv_exp_f32 v137, v137\nv_smfmac_f32_32x32x32_bf16 v[56:71], v[204:207], v[128:135], v43\nv_exp_f32 v138, v138\n" \
    "v_exp_f32 v139, v139\nv_exp_f32 v140, v140\nv_exp_f32 v141, v141\nv_mfma_f32_32x32x16_bf16 v[88:103], v[168:171], v[44:47], v[88:103]\n" \
    "v_exp_f32 v142, v142\nv_exp_f32 v143, v143\nv_exp_f32 v144, v144\nv_exp_f32 v145, v145\n" \
    "v_mfma_f32_32x32x16_bf16 v[104:119], v[172:175], v[44:47], v[104:119]\nv_exp_f32 v146, v146\nv_exp_f32 v147, v147\nv_exp_f32 v148, v148\n" \
    "v_exp_f32 v149, v149\nv_smfmac_f32_32x32x32_bf16 v[56:71], v[208:211], v[136:143], v43\nv_exp_f32 v150, v150\nv_exp_f32 v151, v151\n" \
    "v_exp_f32 v88, v88\nv_exp_f32 v89, v89\nv_smfmac_f32_32x32x32_bf16 v[56:71], v[212:215], v[144:151], v43\nds_read_b128 v[50:53], v7 offset:1280\n" \
    "v_exp_f32 v90, v90\nv_exp_f32 v91, v91\nv_exp_f32 v92, v92\nv_exp_f32 v93, v93\nv_mfma_f32_32x32x4_2b_bf16 v[120:151], v[254:255], v[48:49], 0\n" \
    "v_exp_f32 v94, v94\nv_exp_f32 v95, v95\nv_exp_f32 v96, v96\nv_exp_f32 v97, v97\nv_exp_f32 v98, v98\nv_exp_f32 v99, v99\nv_exp_f32 v100, v100\n" \
    "v_exp_f32 v101, v101\nv_smfmac_f32_32x32x32_bf16 v[56:71], v[216:219], v[88:95], v43\nds_read_b64 v[54:55], v11 offset:1280\nv_exp_f32 v102, v102\n" \
    "v_exp_f32 v103, v103\nv_exp_f32 v104, v104\nv_exp_f32 v105, v105\nv_smfmac_f32_32x32x32_bf16 v[56:71], v[220:223], v[96:103], v43\n" \
    "v_exp_f32 v106, v106\nv_exp_f32 v107, v107\nv_exp_f32 v108, v108\nv_exp_f32 v109, v109\n" \
    "v_mfma_f32_32x32x16_bf16 v[120:135], v[176:179], v[44:47], v[120:135]\nv_exp_f32 v110, v110\nv_exp_f32 v111, v111\nv_exp_f32 v112, v112\n" \
    "v_exp_f32 v113, v113\nv_mfma_f32_32x32x16_bf16 v[136:151], v[180:183], v[44:47], v[136:151]\nv_exp_f32 v114, v114\nv_exp_f32 v115, v115\n" \
    "v_exp_f32 v116, v116\nv_exp_f32 v117, v117\nv_smfmac_f32_32x32x32_bf16 v[56:71], v[224:227], v[104:111], v43\nv_exp_f32 v118, v118\n" \
    "v_exp_f32 v119, v119\nv_exp_f32 v120, v120\nv_exp_f32 v121, v121\nv_smfmac_f32_32x32x32_bf16 v[56:71], v[228:231], v[112:119], v43\n" \
    "ds_read_b32 v41, %[f0] offset:4\ns_waitcnt lgkmcnt(1)\nv_exp_f32 v122, v122\nv_exp_f32 v123, v123\nv_exp_f32 v124, v124\nv_exp_f32_e64 v125, v125\n" \
    "v_mfma_f32_32x32x4_2b_bf16 v[88:119], v[248:249], v[54:55], 0\nv_exp_f32 v126, v126\nv_exp_f32 v127, v127\nv_exp_f32 v128, v128\n" \
    "v_exp_f32 v129, v129\nv_exp_f32 v130, v130\nv_exp_f32 v131, v131\nv_exp_f32 v132, v132\nv_exp_f32 v133, v133\n" \
    "v_smfmac_f32_32x32x32_bf16 v[56:71], v[232:235], v[120:127], v43\nds_read_b128 v[72:75], v3 offset:12288\nv_exp_f32 v134, v134\nv_exp_f32 v135, v135\n" \
    "v_exp_f32 v136, v136\nv_exp_f32 v137, v137\nv_smfmac_f32_32x32x32_bf16 v[56:71], v[236:239], v[128:135], v43\nds_read_b128 v[76:79], v3 offset:13312\n" \
    "s_waitcnt lgkmcnt(2)\nv_exp_f32 v138, v138\nv_exp_f32 v139, v139\nv_readfirstlane_b32 %[st], v41\nv_exp_f32 v140, v140\nv_exp_f32 v141, v141\n" \
    "v_mfma_f32_32x32x16_bf16 v[88:103], v[152:155], v[50:53], v[88:103]\nds_read_b128 v[80:83], v3 offset:14336\nv_exp_f32 v142, v142\n" \
    "v_exp_f32 v143, v143\nv_exp_f32 v144, v144\nv_exp_f32 v145, v145\nv_mfma_f32_32x32x16_bf16 v[104:119], v[156:159], v[50:53], v[104:119]\n" \
    "s_cmp_ge_u32 %[st], 2\ns_cbranch_scc0 .Lrt2_%=\n.Lok2_%=:\nv_exp_f32 v146, v146\nv_exp_f32 v147, v147\nv_exp_f32 v148, v148\nv_exp_f32 v149, v149\n" \
    "v_smfmac_f32_32x32x32_bf16 v[56:71], v[240:243], v[136:143], v43\nv_exp_f32 v150, v150\nv_exp_f32 v151, v151\n" \
    "v_exp_f32 v88, v88\nv_exp_f32 v89, v89\nv_smfmac_f32_32x32x32_bf16 v[56:71], v[244:247], v[144:151], v43\nv_exp_f32 v90, v90\nv_exp_f32 v91, v91\n" \
    "v_exp_f32 v92, v92\nv_exp_f32 v93, v93\nv_mfma_f32_32x32x4_2b_bf16 v[120:151], v[250:251], v[54:55], 0\ns_waitcnt lgkmcnt(0)\nv_exp_f32 v94, v94\n" \
    "v_exp_f32 v95, v95\nv_exp_f32 v96, v96\nv_exp_f32 v97, v97\nv_mov_b32 v42, 3\nv_exp_f32 v98, v98\nv_exp_f32 v99, v99\nv_exp_f32 v100, v100\n" \
    "v_exp_f32 v101, v101\nv_smfmac_f32_32x32x32_bf16 v[72:87], v[184:187], v[88:95], v43\nds_write_b128 v3, v[56:59] offset:0\nv_exp_f32 v102, v102\n" \
    "v_exp_f32 v103, v103\nv_exp_f32 v104, v104\nv_exp_f32 v105, v105\nv_smfmac_f32_32x32x32_bf16 v[72:87], v[188:191], v[96:103], v43\n" \
    "ds_write_b128 v3, v[60:63] offset:1024\nv_exp_f32 v106, v106\nv_exp_f32 v107, v107\nv_exp_f32 v108, v108\nv_exp_f32 v109, v109\n" \
    "v_mfma_f32_32x32x16_bf16 v[120:135], v[160:163], v[50:53], v[120:135]\nds_write_b128 v3, v[64:67] offset:2048\nv_exp_f32 v110, v110\n" \
    "v_exp_f32 v111, v111\nv_exp_f32 v112, v112\nv_exp_f32 v113, v113\nv_mfma_f32_32x32x16_bf16 v[136:151], v[164:167], v[50:53], v[136:151]\n" \
    "ds_write2_b32 %[f0], v42, v42 offset0:0 offset1:4\nv_exp_f32 v114, v114\nv_exp_f32 v115, v115\nv_exp_f32 v116, v116\nv_exp_f32 v117, v117\n" \
    "v_smfmac_f32_32x32x32_bf16 v[72:87], v[192:195], v[104:111], v43\nv_exp_f32 v118, v118\nv_exp_f32 v119, v119\nv_exp_f32 v120, v120\n" \
    "v_exp_f32 v121, v121\nv_smfmac_f32_32x32x32_bf16 v[72:87], v[196:199], v[112:119], v43\nv_exp_f32 v122, v122\nv_exp_f32 v123, v123\n" \
    "v_exp_f32 v124, v124\nv_exp_f32 v125, v125\nv_mfma_f32_32x32x4_2b_bf16 v[88:119], v[252:253], v[54:55], 0\nv_exp_f32 v126, v126\n" \
    "v_exp_f32 v127, v127\nv_exp_f32 v128, v128\nv_exp_f32 v129, v129\nv_exp_f32 v130, v130\nv_exp_f32 v131, v131\nv_exp_f32 v132, v132\n" \
    "v_exp_f32 v133, v133\nv_smfmac_f32_32x32x32_bf16 v[72:87], v[200:203], v[120:127], v43\nv_exp_f32 v134, v134\nv_exp_f32 v135, v135\n" \
    "v_exp_f32 v136, v136\nv_exp_f32 v137, v137\nv_smfmac_f32_32x32x32_bf16 v[72:87], v[204:207], v[128:135], v43\nv_exp_f32 v138, v138\n" \
    "v_exp_f32 v139, v139\nv_exp_f32 v140, v140\nv_exp_f32 v141, v141\nv_mfma_f32_32x32x16_bf16 v[88:103], v[168:171], v[50:53], v[88:103]\n" \
    "v_exp_f32 v142, v142\nv_exp_f32 v143, v143\nv_exp_f32 v144, v144\nv_exp_f32 v145, v145\n" \
    "v_mfma_f32_32x32x16_bf16 v[104:119], v[172:175], v[50:53], v[104:119]\nv_exp_f32 v146, v146\nv_exp_f32 v147, v147\nv_exp_f32 v148, v148\n" \
    "v_exp_f32 v149, v149\nv_smfmac_f32_32x32x32_bf16 v[72:87], v[208:211], v[136:143], v43\nv_exp_f32 v150, v150\nv_exp_f32 v151, v151\n" \
    "v_exp_f32 v88, v88\nv_exp_f32 v89, v89\nv_smfmac_f32_32x32x32_bf16 v[72:87], v[212:215], v[144:151], v43\nds_read_b128 v[44:47], v8 offset:0\n" \
    "v_exp_f32 v90, v90\nv_exp_f32 v91, v91\nv_exp_f32 v92, v92\nv_exp_f32 v93, v93\nv_mfma_f32_32x32x4_2b_bf16 v[120:151], v[254:255], v[54:55], 0\n" \
    "v_exp_f32 v94, v94\nv_exp_f32 v95, v95\nv_exp_f32 v96, v96\nv_exp_f32 v97, v97\nv_exp_f32 v98, v98\nv_exp_f32 v99, v99\nv_exp_f32 v100, v100\n" \
    "v_exp_f32 v101, v101\nv_smfmac_f32_32x32x32_bf16 v[72:87], v[216:219], v[88:95], v43\nds_read_b64 v[48:49], v12 offset:0\nv_exp_f32 v102, v102\n" \
    "v_exp_f32 v103, v103\nv_exp_f32 v104, v104\nv_exp_f32 v105, v105\nv_smfmac_f32_32x32x32_bf16 v[72:87], v[220:223], v[96:103], v43\n" \
    "v_exp_f32 v106, v106\nv_exp_f32 v107, v107\nv_exp_f32 v108, v108\nv_exp_f32 v109, v109\n" \
    "v_mfma_f32_32x32x16_bf16 v[120:135], v[176:179], v[50:53], v[120:135]\nv_exp_f32 v110, v110\nv_exp_f32 v111, v111\nv_exp_f32 v112, v112\n" \
    "v_exp_f32 v113, v113\nv_mfma_f32_32x32x16_bf16 v[136:151], v[180:183], v[50:53], v[136:151]\nv_exp_f32 v114, v114\nv_exp_f32 v115, v115\n" \
    "v_exp_f32 v116, v116\nv_exp_f32 v117, v117\nv_smfmac_f32_32x32x32_bf16 v[72:87], v[224:227], v[104:111], v43\nv_exp_f32 v118, v118\n" \
    "v_exp_f32 v119, v119\nv_exp_f32 v120, v120\nv_exp_f32 v121, v121\nv_smfmac_f32_32x32x32_bf16 v[72:87], v[228:231], v[112:119], v43\n" \
    "ds_read_b32 v41, %[f0] offset:4\ns_waitcnt lgkmcnt(1)\nv_exp_f32 v122, v122\nv_exp_f32 v123, v123\nv_exp_f32 v124, v124\nv_exp_f32_e64 v125, v125\n" \
    "v_mfma_f32_32x32x4_2b_bf16 v[88:119], v[248:249], v[48:49], 0\nv_exp_f32 v126, v126\nv_exp_f32 v127, v127\nv_exp_f32 v128, v128\n" \
    "v_exp_f32 v129, v129\nv_exp_f32 v130, v130\nv_exp_f32 v131, v131\nv_exp_f32 v132, v132\nv_exp_f32 v133, v133\n" \
    "v_smfmac_f32_32x32x32_bf16 v[72:87], v[232:235], v[120:127], v43\nds_read_b128 v[56:59], v4 offset:0\nv_exp_f32 v134, v134\nv_exp_f32 v135, v135\n" \
    "v_exp_f32 v136, v136\nv_exp_f32 v137, v137\nv_smfmac_f32_32x32x32_bf16 v[72:87], v[236:239], v[128:135], v43\nds_read_b128 v[60:63], v4 offset:1024\n" \
    "s_waitcnt lgkmcnt(2)\nv_exp_f32 v138, v138\nv_exp_f32 v139, v139\nv_readfirstlane_b32 %[st], v41\nv_exp_f32 v140, v140\nv_exp_f32 v141, v141\n" \
    "v_mfma_f32_32x32x16_bf16 v[88:103], v[152:155], v[44:47], v[88:103]\nds_read_b128 v[64:67], v4 offset:2048\nv_exp_f32 v142, v142\n" \
    "v_exp_f32 v143, v143\nv_exp_f32 v144, v144\nv_exp_f32 v145, v145\nv_mfma_f32_32x32x16_bf16 v[104:119], v[156:159], v[44:47], v[104:119]\n" \
    "s_cmp_ge_u32 %[st], 3\ns_cbranch_scc0 .Lrt3_%=\n.Lok3_%=:\nv_exp_f32 v146, v146\nv_exp_f32 v147, v147\nv_exp_f32 v148, v148\nv_exp_f32 v149, v149\n" \
    "v_smfmac_f32_32x32x32_bf16 v[72:87], v[240:243], v[136:143], v43\nv_exp_f32 v150, v150\nv_exp_f32 v151, v151\n" \
    "v_exp_f32 v88, v88\nv_exp_f32 v89, v89\nv_smfmac_f32_32x32x32_bf16 v[72:87], v[244:247], v[144:151], v43\nv_exp_f32 v90, v90\nv_exp_f32 v91, v91\n" \
    "v_exp_f32 v92, v92\nv_exp_f32 v93, v93\nv_mfma_f32_32x32x4_2b_bf16 v[120:151], v[250:251], v[48:49], 0\ns_waitcnt lgkmcnt(0)\nv_exp_f32 v94, v94\n" \
    "v_exp_f32 v95, v95\nv_exp_f32 v96, v96\nv_exp_f32 v97, v97\nv_mov_b32 v42, 4\nv_exp_f32 v98, v98\nv_exp_f32 v99, v99\nv_exp_f32 v100, v100\n" \
    "v_exp_f32 v101, v101\nv_smfmac_f32_32x32x32_bf16 v[56:71], v[184:187], v[88:95], v43\nds_write_b128 v3, v[72:75] offset:12288\nv_exp_f32 v102, v102\n" \
    "v_exp_f32 v103, v103\nv_exp_f32 v104, v104\nv_exp_f32 v105, v105\nv_smfmac_f32_32x32x32_bf16 v[56:71], v[188:191], v[96:103], v43\n" \
    "ds_write_b128 v3, v[76:79] offset:13312\nv_exp_f32 v106, v106\nv_exp_f32 v107, v107\nv_exp_f32 v108, v108\nv_exp_f32 v109, v109\n" \
    "v_mfma_f32_32x32x16_bf16 v[120:135], v[160:163], v[44:47], v[120:135]\nds_write_b128 v3, v[80:83] offset:14336\nv_exp_f32 v110, v110\n" \
    "v_exp_f32 v111, v111\nv_exp_f32 v112, v112\nv_exp_f32 v113, v113\nv_mfma_f32_32x32x16_bf16 v[136:151], v[164:167], v[44:47], v[136:151]\n" \
    "ds_write2_b32 %[f0], v42, v42 offset0:0 offset1:4\nv_exp_f32 v114, v114\nv_exp_f32 v115, v115\nv_exp_f32 v116, v116\nv_exp_f32 v117, v117\n" \
    "v_smfmac_f32_32x32x32_bf16 v[56:71], v[192:195], v[104:111], v43\nv_exp_f32 v118, v118\nv_exp_f32 v119, v119\nv_exp_f32 v120, v120\n" \
    "v_exp_f32 v121, v121\nv_smfmac_f32_32x32x32_bf16 v[56:71], v[196:199], v[112:119], v43\nv_exp_f32 v122, v122\nv_exp_f32 v123, v123\n" \
    "v_exp_f32 v124, v124\nv_exp_f32 v125, v125\nv_mfma_f32_32x32x4_2b_bf16 v[88:119], v[252:253], v[48:49], 0\nv_exp_f32 v126, v126\n" \
    "v_exp_f32 v127, v127\nv_exp_f32 v128, v128\nv_exp_f32 v129, v129\nv_exp_f32 v130, v130\nv_exp_f32 v131, v131\nv_exp_f32 v132, v132\n" \
    "v_exp_f32 v133, v133\nv_smfmac_f32_32x32x32_bf16 v[56:71], v[200:203], v[120:127], v43\nv_exp_f32 v134, v134\nv_exp_f32 v135, v135\n" \
    "v_exp_f32 v136, v136\nv_exp_f32 v137, v137\nv_smfmac_f32_32x32x32_bf16 v[56:71], v[204:207], v[128:135], v43\nv_exp_f32 v138, v138\n" \
    "v_exp_f32 v139, v139\nv_exp_f32 v140, v140\nv_exp_f32 v141, v141\nv_mfma_f32_32x32x16_bf16 v[88:103], v[168:171], v[44:47], v[88:103]\n" \
    "v_exp_f32 v142, v142\nv_exp_f32 v143, v143\nv_exp_f32 v144, v144\nv_exp_f32 v145, v145\n" \
    "v_mfma_f32_32x32x16_bf16 v[104:119], v[172:175], v[44:47], v[104:119]\nv_exp_f32 v146, v146\nv_exp_f32 v147, v147\nv_exp_f32 v148, v148\n" \
    "v_exp_f32 v149, v149\nv_smfmac_f32_32x32x32_bf16 v[56:71], v[208:211], v[136:143], v43\nv_exp_f32 v150, v150\nv_exp_f32 v151, v151\n" \
    "v_exp_f32 v88, v88\nv_exp_f32 v89, v89\nv_smfmac_f32_32x32x32_bf16 v[56:71], v[212:215], v[144:151], v43\nds_read_b128 v[50:53], v8 offset:1280\n" \
    "v_exp_f32 v90, v90\nv_exp_f32 v91, v91\nv_exp_f32 v92, v92\nv_exp_f32 v93, v93\nv_mfma_f32_32x32x4_2b_bf16 v[120:151], v[254:255], v[48:49], 0\n" \
    "v_exp_f32 v94, v94\nv_exp_f32 v95, v95\nv_exp_f32 v96, v96\nv_exp_f32 v97, v97\nv_exp_f32 v98, v98\nv_exp_f32 v99, v99\nv_exp_f32 v100, v100\n" \
    "v_exp_f32 v101, v101\nv_smfmac_f32_32x32x32_bf16 v[56:71], v[216:219], v[88:95], v43\nds_read_b64 v[54:55], v12 offset:1280\nv_exp_f32 v102, v102\n" \
    "v_exp_f32 v103, v103\nv_exp_f32 v104, v104\nv_exp_f32 v105, v105\nv_smfmac_f32_32x32x32_bf16 v[56:71], v[220:223], v[96:103], v43\n" \
    "v_exp_f32 v106, v106\nv_exp_f32 v107, v107\nv_exp_f32 v108, v108\nv_exp_f32 v109, v109\n" \
    "v_mfma_f32_32x32x16_bf16 v[120:135], v[176:179], v[44:47], v[120:135]\nv_exp_f32 v110, v110\nv_exp_f32 v111, v111\nv_exp_f32 v112, v112\n" \
    "v_exp_f32 v113, v113\nv_mfma_f32_32x32x16_bf16 v[136:151], v[180:183], v[44:47], v[136:151]\nv_exp_f32 v114, v114\nv_exp_f32 v115, v115\n" \
    "v_exp_f32 v116, v116\nv_exp_f32 v117, v117\nv_smfmac_f32_32x32x32_bf16 v[56:71], v[224:227], v[104:111], v43\nv_exp_f32 v118, v118\n" \
    "v_exp_f32 v119, v119\nv_exp_f32 v120, v120\nv_exp_f32 v121, v121\nv_smfmac_f32_32x32x32_bf16 v[56:71], v[228:231], v[112:119], v43\n" \
    "ds_read_b32 v41, %[f0] offset:4\ns_waitcnt lgkmcnt(1)\nv_exp_f32 v122, v122\nv_exp_f32 v123, v123\nv_exp_f32 v124, v124\nv_exp_f32_e64 v125, v125\n" \
    "v_mfma_f32_32x32x4_2b_bf16 v[88:119], v[248:249], v[54:55], 0\nv_exp_f32 v126, v126\nv_exp_f32 v127, v127\nv_exp_f32 v128, v128\n" \
    "v_exp_f32 v129, v129\nv_exp_f32 v130, v130\nv_exp_f32 v131, v131\nv_exp_f32 v132, v132\nv_exp_f32 v133, v133\n" \
    "v_smfmac_f32_32x32x32_bf16 v[56:71], v[232:235], v[120:127], v43\nds_read_b128 v[72:75], v4 offset:12288\nv_exp_f32 v134, v134\nv_exp_f32 v135, v135\n" \
    "v_exp_f32 v136, v136\nv_exp_f32 v137, v137\nv_smfmac_f32_32x32x32_bf16 v[56:71], v[236:239], v[128:135], v43\nds_read_b128 v[76:79], v4 offset:13312\n" \
    "s_waitcnt lgkmcnt(2)\nv_exp_f32 v138, v138\nv_exp_f32 v139, v139\nv_readfirstlane_b32 %[st], v41\nv_exp_f32 v140, v140\nv_exp_f32 v141, v141\n" \
    "v_mfma_f32_32x32x16_bf16 v[88:103], v[152:155], v[50:53], v[88:103]\nds_read_b128 v[80:83], v4 offset:14336\nv_exp_f32 v142, v142\n" \
    "v_exp_f32 v143, v143\nv_exp_f32 v144, v144\nv_exp_f32 v145, v145\nv_mfma_f32_32x32x16_bf16 v[104:119], v[156:159], v[50:53], v[104:119]\n" \
    "s_cmp_ge_u32 %[st], 4\ns_cbranch_scc0 .Lrt4_%=\n.Lok4_%=:\nv_exp_f32 v146, v146\nv_exp_f32 v147, v147\nv_exp_f32 v148, v148\nv_exp_f32 v149, v149\n" \
    "v_smfmac_f32_32x32x32_bf16 v[56:71], v[240:243], v[136:143], v43\nv_exp_f32 v150, v150\nv_exp_f32 v151, v151\n" \
    "v_exp_f32 v88, v88\nv_exp_f32 v89, v89\nv_smfmac_f32_32x32x32_bf16 v[56:71], v[244:247], v[144:151], v43\nv_exp_f32 v90, v90\nv_exp_f32 v91, v91\n" \
    "v_exp_f32 v92, v92\nv_exp_f32 v93, v93\nv_mfma_f32_32x32x4_2b_bf16 v[120:151], v[250:251], v[54:55], 0\ns_waitcnt lgkmcnt(0)\nv_exp_f32 v94, v94\n" \
    "v_exp_f32 v95, v95\nv_exp_f32 v96, v96\nv_exp_f32 v97, v97\nv_mov_b32 v42, 5\nv_exp_f32 v98, v98\nv_exp_f32 v99, v99\nv_exp_f32 v100, v100\n" \
    "v_exp_f32 v101, v101\nv_smfmac_f32_32x32x32_bf16 v[72:87], v[184:187], v[88:95], v43\nds_write_b128 v4, v[56:59] offset:0\nv_exp_f32 v102, v102\n" \
    "v_exp_f32 v103, v103\nv_exp_f32 v104, v104\nv_exp_f32 v105, v105\nv_smfmac_f32_32x32x32_bf16 v[72:87], v[188:191], v[96:103], v43\n" \
    "ds_write_b128 v4, v[60:63] offset:1024\nv_exp_f32 v106, v106\nv_exp_f32 v107, v107\nv_exp_f32 v108, v108\nv_exp_f32 v109, v109\n" \
    "v_mfma_f32_32x32x16_bf16 v[120:135], v[160:163], v[50:53], v[120:135]\nds_write_b128 v4, v[64:67] offset:2048\nv_exp_f32 v110, v110\n" \
    "v_exp_f32 v111, v111\nv_exp_f32 v112, v112\nv_exp_f32 v113, v113\nv_mfma_f32_32x32x16_bf16 v[136:151], v[164:167], v[50:53], v[136:151]\n" \
    "ds_write2_b32 %[f0], v42, v42 offset0:0 offset1:4\nv_exp_f32 v114, v114\nv_exp_f32 v115, v115\nv_exp_f32 v116, v116\nv_exp_f32 v117, v117\n" \
    "v_smfmac_f32_32x32x32_bf16 v[72:87], v[192:195], v[104:111], v43\nv_exp_f32 v118, v118\nv_exp_f32 v119, v119\nv_exp_f32 v120, v120\n" \
    "v_exp_f32 v121, v121\nv_smfmac_f32_32x32x32_bf16 v[72:87], v[196:199], v[112:119], v43\nv_exp_f32 v122, v122\nv_exp_f32 v123, v123\n" \
    "v_exp_f32 v124, v124\nv_exp_f32 v125, v125\nv_mfma_f32_32x32x4_2b_bf16 v[88:119], v[252:253], v[54:55], 0\nv_exp_f32 v126, v126\n" \
    "v_exp_f32 v127, v127\nv_exp_f32 v128, v128\nv_exp_f32 v129, v129\nv_exp_f32 v130, v130\nv_exp_f32 v131, v131\nv_exp_f32 v132, v132\n" \
    "v_exp_f32 v133, v133\nv_smfmac_f32_32x32x32_bf16 v[72:87], v[200:203], v[120:127], v43\nv_exp_f32 v134, v134\nv_exp_f32 v135, v135\n" \
    "v_exp_f32 v136, v136\nv_exp_f32 v137, v137\nv_smfmac_f32_32x32x32_bf16 v[72:87], v[204:207], v[128:135], v43\nv_exp_f32 v138, v138\n" \
    "v_exp_f32 v139, v139\nv_exp_f32 v140, v140\nv_exp_f32 v141, v141\nv_mfma_f32_32x32x16_bf16 v[88:103], v[168:171], v[50:53], v[88:103]\n" \
    "v_exp_f32 v142, v142\nv_exp_f32 v143, v143\nv_exp_f32 v144, v144\nv_exp_f32 v145, v145\n" \
    "v_mfma_f32_32x32x16_bf16 v[104:119], v[172:175], v[50:53], v[104:119]\nv_exp_f32 v146, v146\nv_exp_f32 v147, v147\nv_exp_f32 v148, v148\n" \
    "v_exp_f32 v149, v149\nv_smfmac_f32_32x32x32_bf16 v[72:87], v[208:211], v[136:143], v43\nv_exp_f32 v150, v150\nv_exp_f32 v151, v151\n" \
    "v_exp_f32 v88, v88\nv_exp_f32 v89, v89\nv_smfmac_f32_32x32x32_bf16 v[72:87], v[212:215], v[144:151], v43\nds_read_b128 v[44:47], v9 offset:0\n" \
    "v_exp_f32 v90, v90\nv_exp_f32 v91, v91\nv_exp_f32 v92, v92\nv_exp_f32 v93, v93\nv_mfma_f32_32x32x4_2b_bf16 v[120:151], v[254:255], v[54:55], 0\n" \
    "v_exp_f32 v94, v94\nv_exp_f32 v95, v95\nv_exp_f32 v96, v96\nv_exp_f32 v97, v97\nv_exp_f32 v98, v98\nv_exp_f32 v99, v99\nv_exp_f32 v100, v100\n" \
    "v_exp_f32 v101, v101\nv_smfmac_f32_32x32x32_bf16 v[72:87], v[216:219], v[88:95], v43\nds_read_b64 v[48:49], v13 offset:0\nv_exp_f32 v102, v102\n" \
    "v_exp_f32 v103, v103\nv_exp_f32 v104, v104\nv_exp_f32 v105, v105\nv_smfmac_f32_32x32x32_bf16 v[72:87], v[220:223], v[96:103], v43\n" \
    "v_exp_f32 v106, v106\nv_exp_f32 v107, v107\nv_exp_f32 v108, v108\nv_exp_f32 v109, v109\n" \
    "v_mfma_f32_32x32x16_bf16 v[120:135], v[176:179], v[50:53], v[120:135]\nv_exp_f32 v110, v110\nv_exp_f32 v111, v111\nv_exp_f32 v112, v112\n" \
    "v_exp_f32 v113, v113\nv_mfma_f32_32x32x16_bf16 v[136:151], v[180:183], v[50:53], v[136:151]\nv_exp_f32 v114, v114\nv_exp_f32 v115, v115\n" \
    "v_exp_f32 v116, v116\nv_exp_f32 v117, v117\nv_smfmac_f32_32x32x32_bf16 v[72:87], v[224:227], v[104:111], v43\nv_exp_f32 v118, v118\n" \
    "v_exp_f32 v119, v119\nv_exp_f32 v120, v120\nv_exp_f32 v121, v121\nv_smfmac_f32_32x32x32_bf16 v[72:87], v[228:231], v[112:119], v43\n" \
    "ds_read_b32 v41, %[f0] offset:4\ns_waitcnt lgkmcnt(1)\nv_exp_f32 v122, v122\nv_exp_f32 v123, v123\nv_exp_f32 v124, v124\nv_exp_f32_e64 v125, v125\n" \
    "v_mfma_f32_32x32x4_2b_bf16 v[88:119], v[248:249], v[48:49], 0\nv_exp_f32 v126, v126\nv_exp_f32 v127, v127\nv_exp_f32 v128, v128\n" \
    "v_exp_f32 v129, v129\nv_exp_f32 v130, v130\nv_exp_f32 v131, v131\nv_exp_f32 v132, v132\nv_exp_f32 v133, v133\n" \
    "v_smfmac_f32_32x32x32_bf16 v[72:87], v[232:235], v[120:127], v43\nds_read_b128 v[56:59], v5 offset:0\nv_exp_f32 v134, v134\nv_exp_f32 v135, v135\n" \
    "v_exp_f32 v136, v136\nv_exp_f32 v137, v137\nv_smfmac_f32_32x32x32_bf16 v[72:87], v[236:239], v[128:135], v43\nds_read_b128 v[60:63], v5 offset:1024\n" \
    "s_waitcnt lgkmcnt(2)\nv_exp_f32 v138, v138\nv_exp_f32 v139, v139\nv_readfirstlane_b32 %[st], v41\nv_exp_f32 v140, v140\nv_exp_f32 v141, v141\n" \
    "v_mfma_f32_32x32x16_bf16 v[88:103], v[152:155], v[44:47], v[88:103]\nds_read_b128 v[64:67], v5 offset:2048\nv_exp_f32 v142, v142\n" \
    "v_exp_f32 v143, v143\nv_exp_f32 v144, v144\nv_exp_f32 v145, v145\nv_mfma_f32_32x32x16_bf16 v[104:119], v[156:159], v[44:47], v[104:119]\n" \
    "s_cmp_ge_u32 %[st], 5\ns_cbranch_scc0 .Lrt5_%=\n.Lok5_%=:\nv_exp_f32 v146, v146\nv_exp_f32 v147, v147\nv_exp_f32 v148, v148\nv_exp_f32 v149, v149\n" \
    "v_smfmac_f32_32x32x32_bf16 v[72:87], v[240:243], v[136:143], v43\nv_exp_f32 v150, v150\nv_exp_f32 v151, v151\n" \
    "v_exp_f32 v88, v88\nv_exp_f32 v89, v89\nv_smfmac_f32_32x32x32_bf16 v[72:87], v[244:247], v[144:151], v43\nv_exp_f32 v90, v90\nv_exp_f32 v91, v91\n" \
    "v_exp_f32 v92, v92\nv_exp_f32 v93, v93\nv_mfma_f32_32x32x4_2b_bf16 v[120:151], v[250:251], v[48:49], 0\ns_waitcnt lgkmcnt(0)\nv_exp_f32 v94, v94\n" \
    "v_exp_f32 v95, v95\nv_exp_f32 v96, v96\nv_exp_f32 v97, v97\nv_mov_b32 v42, 6\nv_exp_f32 v98, v98\nv_exp_f32 v99, v99\nv_exp_f32 v100, v100\n" \
    "v_exp_f32 v101, v101\nv_smfmac_f32_32x32x32_bf16 v[56:71], v[184:187], v[88:95], v43\nds_write_b128 v4, v[72:75] offset:12288\nv_exp_f32 v102, v102\n" \
    "v_exp_f32 v103, v103\nv_exp_f32 v104, v104\nv_exp_f32 v105, v105\nv_smfmac_f32_32x32x32_bf16 v[56:71], v[188:191], v[96:103], v43\n" \
    "ds_write_b128 v4, v[76:79] offset:13312\nv_exp_f32 v106, v106\nv_exp_f32 v107, v107\nv_exp_f32 v108, v108\nv_exp_f32 v109, v109\n" \
    "v_mfma_f32_32x32x16_bf16 v[120:135], v[160:163], v[44:47], v[120:135]\nds_write_b128 v4, v[80:83] offset:14336\nv_exp_f32 v110, v110\n" \
    "v_exp_f32 v111, v111\nv_exp_f32 v112, v112\nv_exp_f32 v113, v113\nv_mfma_f32_32x32x16_bf16 v[136:151], v[164:167], v[44:47], v[136:151]\n" \
    "ds_write2_b32 %[f0], v42, v42 offset0:0 offset1:4\nv_exp_f32 v114, v114\nv_exp_f32 v115, v115\nv_exp_f32 v116, v116\nv_exp_f32 v117, v117\n" \
    "v_smfmac_f32_32x32x32_bf16 v[56:71], v[192:195], v[104:111], v43\nv_exp_f32 v118, v118\nv_exp_f32 v119, v119\nv_exp_f32 v120, v120\n" \
    "v_exp_f32 v121, v121\nv_smfmac_f32_32x32x32_bf16 v[56:71], v[196:199], v[112:119], v43\nv_exp_f32 v122, v122\nv_exp_f32 v123, v123\n" \
    "v_exp_f32 v124, v124\nv_exp_f32 v125, v125\nv_mfma_f32_32x32x4_2b_bf16 v[88:119], v[252:253], v[48:49], 0\nv_exp_f32 v126, v126\n" \
    "v_exp_f32 v127, v127\nv_exp_f32 v128, v128\nv_exp_f32 v129, v129\nv_exp_f32 v130, v130\nv_exp_f32 v131, v131\nv_exp_f32 v132, v132\n" \
    "v_exp_f32 v133, v133\nv_smfmac_f32_32x32x32_bf16 v[56:71], v[200:203], v[120:127], v43\nv_exp_f32 v134, v134\nv_exp_f32 v135, v135\n" \
    "v_exp_f32 v136, v136\nv_exp_f32 v137, v137\nv_smfmac_f32_32x32x32_bf16 v[56:71], v[204:207], v[128:135], v43\nv_exp_f32 v138, v138\n" \
    "v_exp_f32 v139, v139\nv_exp_f32 v140, v140\nv_exp_f32 v141, v141\nv_mfma_f32_32x32x16_bf16 v[88:103], v[168:171], v[44:47], v[88:103]\n" \
    "v_exp_f32 v142, v142\nv_exp_f32 v143, v143\nv_exp_f32 v144, v144\nv_exp_f32 v145, v145\n" \
    "v_mfma_f32_32x32x16_bf16 v[104:119], v[172:175], v[44:47], v[104:119]\nv_exp_f32 v146, v146\nv_exp_f32 v147, v147\nv_exp_f32 v148, v148\n" \
    "v_exp_f32 v149, v149\nv_smfmac_f32_32x32x32_bf16 v[56:71], v[208:211], v[136:143], v43\nv_exp_f32 v150, v150\nv_exp_f32 v151, v151\n" \
    "v_exp_f32 v88, v88\nv_exp_f32 v89, v89\nv_smfmac_f32_32x32x32_bf16 v[56:71], v[212:215], v[144:151], v43\nds_read_b128 v[50:53], v9 offset:1280\n" \
    "v_exp_f32 v90, v90\nv_exp_f32 v91, v91\nv_exp_f32 v92, v92\nv_exp_f32 v93, v93\nv_mfma_f32_32x32x4_2b_bf16 v[120:151], v[254:255], v[48:49], 0\n" \
    "v_exp_f32 v94, v94\nv_exp_f32 v95, v95\nv_exp_f32 v96, v96\nv_exp_f32 v97, v97\nv_exp_f32 v98, v98\nv_exp_f32 v99, v99\nv_exp_f32 v100, v100\n" \
    "v_exp_f32 v101, v101\nv_smfmac_f32_32x32x32_bf16 v[56:71], v[216:219], v[88:95], v43\nds_read_b64 v[54:55], v13 offset:1280\nv_exp_f32 v102, v102\n" \
    "v_exp_f32 v103, v103\nv_exp_f32 v104, v104\nv_exp_f32 v105, v105\nv_smfmac_f32_32x32x32_bf16 v[56:71], v[220:223], v[96:103], v43\n" \
    "v_exp_f32 v106, v106\nv_exp_f32 v107, v107\nv_exp_f32 v108, v108\nv_exp_f32 v109, v109\n" \
    "v_mfma_f32_32x32x16_bf16 v[120:135], v[176:179], v[44:47], v[120:135]\nv_exp_f32 v110, v110\nv_exp_f32 v111, v111\nv_exp_f32 v112, v112\n" \
    "v_exp_f32 v113, v113\nv_mfma_f32_32x32x16_bf16 v[136:151], v[180:183], v[44:47], v[136:151]\nv_exp_f32 v114, v114\nv_exp_f32 v115, v115\n" \
    "v_exp_f32 v116, v116\nv_exp_f32 v117, v117\nv_smfmac_f32_32x32x32_bf16 v[56:71], v[224:227], v[104:111], v43\nv_exp_f32 v118, v118\n" \
    "v_exp_f32 v119, v119\nv_exp_f32 v120, v120\nv_exp_f32 v121, v121\nv_smfmac_f32_32x32x32_bf16 v[56:71], v[228:231], v[112:119], v43\n" \
    "ds_read_b32 v41, %[f0] offset:4\ns_waitcnt lgkmcnt(1)\nv_exp_f32 v122, v122\nv_exp_f32 v123, v123\nv_exp_f32 v124, v124\nv_exp_f32_e64 v125, v125\n" \
    "v_mfma_f32_32x32x4_2b_bf16 v[88:119], v[248:249], v[54:55], 0\nv_exp_f32 v126, v126\nv_exp_f32 v127, v127\nv_exp_f32 v128, v128\n" \
    "v_exp_f32 v129, v129\nv_exp_f32 v130, v130\nv_exp_f32 v131, v131\nv_exp_f32 v132, v132\nv_exp_f32 v133, v133\n" \
    "v_smfmac_f32_32x32x32_bf16 v[56:71], v[232:235], v[120:127], v43\nds_read_b128 v[72:75], v5 offset:12288\nv_exp_f32 v134, v134\nv_exp_f32 v135, v135\n" \
    "v_exp_f32 v136, v136\nv_exp_f32 v137, v137\nv_smfmac_f32_32x32x32_bf16 v[56:71], v[236:239], v[128:135], v43\nds_read_b128 v[76:79], v5 offset:13312\n" \
    "s_waitcnt lgkmcnt(2)\nv_exp_f32 v138, v138\nv_exp_f32 v139, v139\nv_readfirstlane_b32 %[st], v41\nv_exp_f32 v140, v140\nv_exp_f32 v141, v141\n" \
    "v_mfma_f32_32x32x16_bf16 v[88:103], v[152:155], v[50:53], v[88:103]\nds_read_b128 v[80:83], v5 offset:14336\nv_exp_f32 v142, v142\n" \
    "v_exp_f32 v143, v143\nv_exp_f32 v144, v144\nv_exp_f32 v145, v145\nv_mfma_f32_32x32x16_bf16 v[104:119], v[156:159], v[50:53], v[104:119]\n" \
    "s_cmp_ge_u32 %[st], 6\ns_cbranch_scc0 .Lrt6_%=\n.Lok6_%=:\nv_exp_f32 v146, v146\nv_exp_f32 v147, v147\nv_exp_f32 v148, v148\nv_exp_f32 v149, v149\n" \
    "v_smfmac_f32_32x32x32_bf16 v[56:71], v[240:243], v[136:143], v43\nv_exp_f32 v150, v150\nv_exp_f32 v151, v151\n" \
    "v_exp_f32 v88, v88\nv_exp_f32 v89, v89\nv_smfmac_f32_32x32x32_bf16 v[56:71], v[244:247], v[144:151], v43\nv_exp_f32 v90, v90\nv_exp_f32 v91, v91\n" \
    "v_exp_f32 v92, v92\nv_exp_f32 v93, v93\nv_mfma_f32_32x32x4_2b_bf16 v[120:151], v[250:251], v[54:55], 0\ns_waitcnt lgkmcnt(0)\nv_exp_f32 v94, v94\n" \
    "v_exp_f32 v95, v95\nv_exp_f32 v96, v96\nv_exp_f32 v97, v97\nv_mov_b32 v42, 7\nv_exp_f32 v98, v98\nv_exp_f32 v99, v99\nv_exp_f32 v100, v100\n" \
    "v_exp_f32 v101, v101\nv_smfmac_f32_32x32x32_bf16 v[72:87], v[184:187], v[88:95], v43\nds_write_b128 v5, v[56:59] offset:0\nv_exp_f32 v102, v102\n" \
    "v_exp_f32 v103, v103\nv_exp_f32 v104, v104\nv_exp_f32 v105, v105\nv_smfmac_f32_32x32x32_bf16 v[72:87], v[188:191], v[96:103], v43\n" \
    "ds_write_b128 v5, v[60:63] offset:1024\nv_exp_f32 v106, v106\nv_exp_f32 v107, v107\nv_exp_f32 v108, v108\nv_exp_f32 v109, v109\n" \
    "v_mfma_f32_32x32x16_bf16 v[120:135], v[160:163], v[50:53], v[120:135]\nds_write_b128 v5, v[64:67] offset:2048\nv_exp_f32 v110, v110\n" \
    "v_exp_f32 v111, v111\nv_exp_f32 v112, v112\nv_exp_f32 v113, v113\nv_mfma_f32_32x32x16_bf16 v[136:151], v[164:167], v[50:53], v[136:151]\n" \
    "ds_write2_b32 %[f0], v42, v42 offset0:0 offset1:4\nv_exp_f32 v114, v114\nv_exp_f32 v115, v115\nv_exp_f32 v116, v116\nv_exp_f32 v117, v117\n" \
    "v_smfmac_f32_32x32x32_bf16 v[72:87], v[192:195], v[104:111], v43\nv_exp_f32 v118, v118\nv_exp_f32 v119, v119\nv_exp_f32 v120, v120\n" \
    "v_exp_f32 v121, v121\nv_smfmac_f32_32x32x32_bf16 v[72:87], v[196:199], v[112:119], v43\nv_exp_f32 v122, v122\nv_exp_f32 v123, v123\n" \
    "v_exp_f32 v124, v124\nv_exp_f32 v125, v125\nv_mfma_f32_32x32x4_2b_bf16 v[88:119], v[252:253], v[54:55], 0\nv_exp_f32 v126, v126\n" \
    "v_exp_f32 v127, v127\nv_exp_f32 v128, v128\nv_exp_f32 v129, v129\nv_exp_f32 v130, v130\nv_exp_f32 v131, v131\nv_exp_f32 v132, v132\n" \
    "v_exp_f32 v133, v133\nv_smfmac_f32_32x32x32_bf16 v[72:87], v[200:203], v[120:127], v43\nv_exp_f32 v134, v134\nv_exp_f32 v135, v135\n" \
    "v_exp_f32 v136, v136\nv_exp_f32 v137, v137\nv_smfmac_f32_32x32x32_bf16 v[72:87], v[204:207], v[128:135], v43\nv_exp_f32 v138, v138\n" \
    "v_exp_f32 v139, v139\nv_exp_f32 v140, v140\nv_exp_f32 v141, v141\nv_mfma_f32_32x32x16_bf16 v[88:103], v[168:171], v[50:53], v[88:103]\n" \
    "v_exp_f32 v142, v142\nv_exp_f32 v143, v143\nv_exp_f32 v144, v144\nv_exp_f32 v145, v145\n" \
    "v_mfma_f32_32x32x16_bf16 v[104:119], v[172:175], v[50:53], v[104:119]\nv_exp_f32 v146, v146\nv_exp_f32 v147, v147\nv_exp_f32 v148, v148\n" \
    "v_exp_f32 v149, v149\nv_smfmac_f32_32x32x32_bf16 v[72:87], v[208:211], v[136:143], v43\nv_exp_f32 v150, v150\nv_exp_f32 v151, v151\n" \
    "v_exp_f32 v88, v88\nv_exp_f32 v89, v89\nv_smfmac_f32_32x32x32_bf16 v[72:87], v[212:215], v[144:151], v43\nv_exp_f32 v90, v90\nv_exp_f32 v91, v91\n" \
    "v_exp_f32 v92, v92\nv_exp_f32 v93, v93\nv_mfma_f32_32x32x4_2b_bf16 v[120:151], v[254:255], v[54:55], 0\nv_exp_f32 v94, v94\nv_exp_f32 v95, v95\n" \
    "v_exp_f32 v96, v96\nv_exp_f32 v97, v97\nv_exp_f32 v98, v98\nv_exp_f32 v99, v99\nv_exp_f32 v100, v100\nv_exp_f32 v101, v101\n" \
    "v_smfmac_f32_32x32x32_bf16 v[72:87], v[216:219], v[88:95], v43\nv_exp_f32 v102, v102\nv_exp_f32 v103, v103\nv_exp_f32 v104, v104\n" \
    "v_exp_f32 v105, v105\nv_smfmac_f32_32x32x32_bf16 v[72:87], v[220:223], v[96:103], v43\nv_exp_f32 v106, v106\nv_exp_f32 v107, v107\n" \
    "v_exp_f32 v108, v108\nv_exp_f32 v109, v109\nv_mfma_f32_32x32x16_bf16 v[120:135], v[176:179], v[50:53], v[120:135]\nv_exp_f32 v110, v110\n" \
    "v_exp_f32 v111, v111\nv_exp_f32 v112, v112\nv_exp_f32 v113, v113\nv_mfma_f32_32x32x16_bf16 v[136:151], v[180:183], v[50:53], v[136:151]\n" \
    "v_exp_f32 v114, v114\nv_exp_f32 v115, v115\nv_exp_f32 v116, v116\nv_exp_f32 v117, v117\n" \
    "v_smfmac_f32_32x32x32_bf16 v[72:87], v[224:227], v[104:111], v43\nv_exp_f32 v118, v118\nv_exp_f32 v119, v119\nv_exp_f32 v120, v120\n" \
    "v_exp_f32 v121, v121\nv_smfmac_f32_32x32x32_bf16 v[72:87], v[228:231], v[112:119], v43\nv_exp_f32 v122, v122\nv_exp_f32 v123, v123\n" \
    "v_exp_f32 v124, v124\nv_exp_f32 v125, v125\nv_exp_f32 v126, v126\nv_exp_f32 v127, v127\nv_exp_f32 v128, v128\nv_exp_f32 v129, v129\n" \
    "v_exp_f32 v130, v130\nv_exp_f32 v131, v131\nv_exp_f32 v132, v132\nv_exp_f32 v133, v133\n" \
    "v_smfmac_f32_32x32x32_bf16 v[72:87], v[232:235], v[120:127], v43\nv_exp_f32 v134, v134\nv_exp_f32 v135, v135\nv_exp_f32 v136, v136\n" \
    "v_exp_f32 v137, v137\nv_smfmac_f32_32x32x32_bf16 v[72:87], v[236:239], v[128:135], v43\nv_exp_f32 v138, v138\nv_exp_f32 v139, v139\n" \
    "v_exp_f32 v140, v140\nv_exp_f32 v141, v141\nv_exp_f32 v142, v142\nv_exp_f32 v143, v143\nv_exp_f32 v144, v144\nv_exp_f32 v145, v145\n" \
    "v_exp_f32 v146, v146\nv_exp_f32 v147, v147\nv_exp_f32 v148, v148\nv_exp_f32 v149, v149\n" \
    "v_smfmac_f32_32x32x32_bf16 v[72:87], v[240:243], v[136:143], v43\nv_exp_f32 v150, v150\nv_exp_f32 v151, v151\ns_nop 1\ns_nop 0\n" \
    "v_smfmac_f32_32x32x32_bf16 v[72:87], v[244:247], v[144:151], v43\ns_waitcnt lgkmcnt(0)\ns_branch .Lend_%=\n.Lrt1_%=:\ns_sleep 2\ns_nop 0\n" \
    "ds_read_b32 v41, %[f0] offset:4\nds_read_b128 v[56:59], v3 offset:0\nds_read_b128 v[60:63], v3 offset:1024\nds_read_b128 v[64:67], v3 offset:2048\n" \
    "s_waitcnt lgkmcnt(0)\nv_readfirstlane_b32 %[st], v41\ns_add_i32 %[cap], %[cap], -1\ns_cmp_le_i32 %[cap], 0\ns_cbranch_scc1 .Lok1_%=\n" \
    "s_cmp_ge_u32 %[st], 1\ns_cbranch_scc0 .Lrt1_%=\ns_branch .Lok1_%=\n.Lrt2_%=:\ns_sleep 2\ns_nop 0\nds_read_b32 v41, %[f0] offset:4\n" \
    "ds_read_b128 v[72:75], v3 offset:12288\nds_read_b128 v[76:79], v3 offset:13312\nds_read_b128 v[80:83], v3 offset:14336\ns_waitcnt lgkmcnt(0)\n" \
    "v_readfirstlane_b32 %[st], v41\ns_add_i32 %[cap], %[cap], -1\ns_cmp_le_i32 %[cap], 0\ns_cbranch_scc1 .Lok2_%=\ns_cmp_ge_u32 %[st], 2\n" \
    "s_cbranch_scc0 .Lrt2_%=\ns_branch .Lok2_%=\n.Lrt3_%=:\ns_sleep 2\ns_nop 0\nds_read_b32 v41, %[f0] offset:4\nds_read_b128 v[56:59], v4 offset:0\n" \
    "ds_read_b128 v[60:63], v4 offset:1024\nds_read_b128 v[64:67], v4 offset:2048\ns_waitcnt lgkmcnt(0)\nv_readfirstlane_b32 %[st], v41\n" \
    "s_add_i32 %[cap], %[cap], -1\ns_cmp_le_i32 %[cap], 0\ns_cbranch_scc1 .Lok3_%=\ns_cmp_ge_u32 %[st], 3\ns_cbranch_scc0 .Lrt3_%=\ns_branch .Lok3_%=\n" \
    ".Lrt4_%=:\ns_sleep 2\ns_nop 0\nds_read_b32 v41, %[f0] offset:4\nds_read_b128 v[72:75], v4 offset:12288\nds_read_b128 v[76:79], v4 offset:13312\n" \
    "ds_read_b128 v[80:83], v4 offset:14336\ns_waitcnt lgkmcnt(0)\nv_readfirstlane_b32 %[st], v41\ns_add_i32 %[cap], %[cap], -1\ns_cmp_le_i32 %[cap], 0\n" \
    "s_cbranch_scc1 .Lok4_%=\ns_cmp_ge_u32 %[st], 4\ns_cbranch_scc0 .Lrt4_%=\ns_branch .Lok4_%=\n.Lrt5_%=:\ns_sleep 2\ns_nop 0\n" \
    "ds_read_b32 v41, %[f0] offset:4\nds_read_b128 v[56:59], v5 offset:0\nds_read_b128 v[60:63], v5 offset:1024\nds_read_b128 v[64:67], v5 offset:2048\n" \
    "s_waitcnt lgkmcnt(0)\nv_readfirstlane_b32 %[st], v41\ns_add_i32 %[cap], %[cap], -1\ns_cmp_le_i32 %[cap], 0\ns_cbranch_scc1 .Lok5_%=\n" \
    "s_cmp_ge_u32 %[st], 5\ns_cbranch_scc0 .Lrt5_%=\ns_branch .Lok5_%=\n.Lrt6_%=:\ns_sleep 2\ns_nop 0\nds_read_b32 v41, %[f0] offset:4\n" \
    "ds_read_b128 v[72:75], v5 offset:12288\nds_read_b128 v[76:79], v5 offset:13312\nds_read_b128 v[80:83], v5 offset:14336\ns_waitcnt lgkmcnt(0)\n" \
    "v_readfirstlane_b32 %[st], v41\ns_add_i32 %[cap], %[cap], -1\ns_cmp_le_i32 %[cap], 0\ns_cbranch_scc1 .Lok6_%=\ns_cmp_ge_u32 %[st], 6\n" \
    "s_cbranch_scc0 .Lrt6_%=\ns_branch .Lok6_%=\n.Lend_%=:\ns_nop 12\nv_mov_b32 v42, 8\nds_write_b128 v5, v[72:75] offset:12288\n" \
    "ds_write_b128 v5, v[76:79] offset:13312\nds_write_b128 v5, v[80:83] offset:14336\nds_write_b32 %[f0], v42\n"
#define ASM_ATTN_B \
    ".p2align 3\nv_mov_b32 v43, 0xdddddddd\nv_mov_b32 v42, 1\nv_mov_b32 v56, 0\nv_mov_b32 v72, 0\nv_mov_b32 v57, 0\nv_mov_b32 v73, 0\nv_mov_b32 v58, 0\n" \
    "v_mov_b32 v74, 0\nv_mov_b32 v59, 0\nv_mov_b32 v75, 0\nv_mov_b32 v60, 0\nv_mov_b32 v76, 0\nv_mov_b32 v61, 0\nv_mov_b32 v77, 0\nv_mov_b32 v62, 0\n" \
    "v_mov_b32 v78, 0\nv_mov_b32 v63, 0\nv_mov_b32 v79, 0\nv_mov_b32 v64, 0\nv_mov_b32 v80, 0\nv_mov_b32 v65, 0\nv_mov_b32 v81, 0\nv_mov_b32 v66, 0\n" \
    "v_mov_b32 v82, 0\nv_mov_b32 v67, 0\nv_mov_b32 v83, 0\n.Lq_%=:\ns_nop 0\nds_read_b32 v41, %[qd]\nds_read_b128 v[44:47], v6 offset:0\n" \
    "ds_read_b64 v[48:49], v10 offset:0\ns_waitcnt lgkmcnt(0)\nv_readfirstlane_b32 %[st], v41\ns_add_i32 %[cap], %[cap], -1\ns_cmp_le_i32 %[cap], 0\n" \
    "s_cbranch_scc1 .Lq2_%=\ns_cmp_ge_u32 %[st], 4\ns_cbranch_scc0 .Lq_%=\n.Lq2_%=:\ns_nop 0\n" \
    "v_mfma_f32_32x32x4_2b_bf16 v[88:119], v[248:249], v[48:49], 0\ns_nop 15\ns_nop 1\n" \
    "v_mfma_f32_32x32x16_bf16 v[88:103], v[152:155], v[44:47], v[88:103]\nv_mfma_f32_32x32x16_bf16 v[104:119], v[156:159], v[44:47], v[104:119]\ns_nop 10\n" \
    "s_nop 0\n" \
    "v_exp_f32 v88, v88\nv_exp_f32 v89, v89\nv_exp_f32 v90, v90\nv_exp_f32 v91, v91\nv_exp_f32 v92, v92\nv_exp_f32 v93, v93\n" \
    "v_mfma_f32_32x32x4_2b_bf16 v[120:151], v[250:251], v[48:49], 0\nv_exp_f32 v94, v94\nv_exp_f32 v95, v95\nv_exp_f32 v96, v96\nv_exp_f32 v97, v97\n" \
    "v_exp_f32 v98, v98\nv_exp_f32 v99, v99\nv_exp_f32 v100, v100\nv_exp_f32 v101, v101\nv_smfmac_f32_32x32x32_bf16 v[56:71], v[184:187], v[88:95], v43\n" \
    "v_exp_f32 v102, v102\nv_exp_f32 v103, v103\nv_exp_f32 v104, v104\nv_exp_f32 v105, v105\n" \
    "v_smfmac_f32_32x32x32_bf16 v[56:71], v[188:191], v[96:103], v43\nv_exp_f32 v106, v106\nv_exp_f32 v107, v107\nv_exp_f32 v108, v108\n" \
    "v_exp_f32 v109, v109\nv_mfma_f32_32x32x16_bf16 v[120:135], v[160:163], v[44:47], v[120:135]\nv_exp_f32 v110, v110\nv_exp_f32 v111, v111\n" \
    "v_exp_f32 v112, v112\nv_exp_f32 v113, v113\nv_mfma_f32_32x32x16_bf16 v[136:151], v[164:167], v[44:47], v[136:151]\nv_exp_f32 v114, v114\n" \
    "v_exp_f32 v115, v115\nv_exp_f32 v116, v116\nv_exp_f32 v117, v117\nv_smfmac_f32_32x32x32_bf16 v[56:71], v[192:195], v[104:111], v43\n" \
    "v_exp_f32 v118, v118\nv_exp_f32 v119, v119\nv_exp_f32 v120, v120\nv_exp_f32 v121, v121\n" \
    "v_smfmac_f32_32x32x32_bf16 v[56:71], v[196:199], v[112:119], v43\nv_exp_f32 v122, v122\nv_exp_f32 v123, v123\nv_exp_f32 v124, v124\n" \
    "v_exp_f32 v125, v125\nv_mfma_f32_32x32x4_2b_bf16 v[88:119], v[252:253], v[48:49], 0\nv_exp_f32 v126, v126\nv_exp_f32 v127, v127\n" \
    "v_exp_f32 v128, v128\nv_exp_f32 v129, v129\nv_exp_f32 v130, v130\nv_exp_f32 v131, v131\nv_exp_f32 v132, v132\nv_exp_f32 v133, v133\n" \
    "v_smfmac_f32_32x32x32_bf16 v[56:71], v[200:203], v[120:127], v43\nv_exp_f32 v134, v134\nv_exp_f32 v135, v135\nv_exp_f32 v136, v136\n" \
    "v_exp_f32 v137, v137\nv_smfmac_f32_32x32x32_bf16 v[56:71], v[204:207], v[128:135], v43\nv_exp_f32 v138, v138\nv_exp_f32 v139, v139\n" \
    "v_exp_f32 v140, v140\nv_exp_f32 v141, v141\nv_mfma_f32_32x32x16_bf16 v[88:103], v[168:171], v[44:47], v[88:103]\nv_exp_f32 v142, v142\n" \
    "v_exp_f32 v143, v143\nv_exp_f32 v144, v144\nv_exp_f32 v145, v145\nv_mfma_f32_32x32x16_bf16 v[104:119], v[172:175], v[44:47], v[104:119]\n" \
    "v_exp_f32 v146, v146\nv_exp_f32 v147, v147\nv_exp_f32 v148, v148\nv_exp_f32 v149, v149\n" \
    "v_smfmac_f32_32x32x32_bf16 v[56:71], v[208:211], v[136:143], v43\nv_exp_f32 v150, v150\nv_exp_f32 v151, v151\nv_exp_f32 v88, v88\nv_exp_f32 v89, v89\n" \
    "v_smfmac_f32_32x32x32_bf16 v[56:71], v[212:215], v[144:151], v43\nds_read_b128 v[50:53], v6 offset:1280\nv_exp_f32 v90, v90\nv_exp_f32 v91, v91\n" \
    "v_exp_f32 v92, v92\nv_exp_f32 v93, v93\nv_mfma_f32_32x32x4_2b_bf16 v[120:151], v[254:255], v[48:49], 0\nv_exp_f32 v94, v94\nv_exp_f32 v95, v95\n" \
    "v_exp_f32 v96, v96\nv_exp_f32 v97, v97\nv_exp_f32 v98, v98\nv_exp_f32 v99, v99\nv_exp_f32 v100, v100\nv_exp_f32 v101, v101\n" \
    "v_smfmac_f32_32x32x32_bf16 v[56:71], v[216:219], v[88:95], v43\nds_read_b64 v[54:55], v10 offset:1280\nv_exp_f32 v102, v102\nv_exp_f32 v103, v103\n" \
    "v_exp_f32 v104, v104\nv_exp_f32 v105, v105\nv_smfmac_f32_32x32x32_bf16 v[56:71], v[220:223], v[96:103], v43\nv_exp_f32 v106, v106\n" \
    "v_exp_f32 v107, v107\nv_exp_f32 v108, v108\nv_exp_f32 v109, v109\nv_mfma_f32_32x32x16_bf16 v[120:135], v[176:179], v[44:47], v[120:135]\n" \
    "v_exp_f32 v110, v110\nv_exp_f32 v111, v111\nv_exp_f32 v112, v112\nv_exp_f32 v113, v113\n" \
    "v_mfma_f32_32x32x16_bf16 v[136:151], v[180:183], v[44:47], v[136:151]\nv_exp_f32 v114, v114\nv_exp_f32 v115, v115\nv_exp_f32 v116, v116\n" \
    "v_exp_f32 v117, v117\nv_smfmac_f32_32x32x32_bf16 v[56:71], v[224:227], v[104:111], v43\ns_waitcnt vmcnt(0)\ns_mov_b64 exec, 1\n" \
    "ds_add_u32 %[qd], v42 offset:12\ns_mov_b64 exec, -1\nv_exp_f32 v118, v118\nv_exp_f32 v119, v119\nv_exp_f32 v120, v120\nv_exp_f32_e64 v121, v121\n" \
    "v_smfmac_f32_32x32x32_bf16 v[56:71], v[228:231], v[112:119], v43\ns_waitcnt lgkmcnt(0)\nv_exp_f32 v122, v122\nv_exp_f32 v123, v123\n" \
    "v_exp_f32 v124, v124\nv_exp_f32_e64 v125, v125\nv_mfma_f32_32x32x4_2b_bf16 v[88:119], v[248:249], v[54:55], 0\nv_exp_f32 v126, v126\n" \
    "v_exp_f32 v127, v127\nv_exp_f32 v128, v128\nv_exp_f32 v129, v129\nv_exp_f32 v130, v130\nv_exp_f32 v131, v131\nv_exp_f32 v132, v132\n" \
    "v_exp_f32 v133, v133\nv_smfmac_f32_32x32x32_bf16 v[56:71], v[232:235], v[120:127], v43\nds_read_b32 v41, %[qd] offset:4\nv_exp_f32 v134, v134\n" \
    "v_exp_f32 v135, v135\nv_exp_f32 v136, v136\nv_exp_f32 v137, v137\nv_smfmac_f32_32x32x32_bf16 v[56:71], v[236:239], v[128:135], v43\n" \
    "v_exp_f32 v138, v138\nv_exp_f32 v139, v139\nv_exp_f32 v140, v140\nv_exp_f32 v141, v141\n" \
    "v_mfma_f32_32x32x16_bf16 v[88:103], v[152:155], v[50:53], v[88:103]\nv_exp_f32 v142, v142\nv_exp_f32 v143, v143\nv_exp_f32 v144, v144\n" \
    "v_exp_f32 v145, v145\nv_mfma_f32_32x32x16_bf16 v[104:119], v[156:159], v[50:53], v[104:119]\ns_waitcnt lgkmcnt(0)\nv_exp_f32 v146, v146\n" \
    "v_exp_f32 v147, v147\nv_readfirstlane_b32 %[st], v41\nv_exp_f32 v148, v148\nv_exp_f32 v149, v149\n" \
    "v_smfmac_f32_32x32x32_bf16 v[56:71], v[240:243], v[136:143], v43\ns_cmp_ge_u32 %[st], 1\ns_cbranch_scc0 .Lrt0_%=\n.Lok0_%=:\nv_exp_f32 v150, v150\n" \
    "v_exp_f32 v151, v151\n" \
    "v_exp_f32 v88, v88\nv_exp_f32 v89, v89\nv_smfmac_f32_32x32x32_bf16 v[56:71], v[244:247], v[144:151], v43\nv_exp_f32 v90, v90\nv_exp_f32 v91, v91\n" \
    "v_exp_f32 v92, v92\nv_exp_f32 v93, v93\nv_mfma_f32_32x32x4_2b_bf16 v[120:151], v[250:251], v[54:55], 0\nv_exp_f32 v94, v94\nv_exp_f32 v95, v95\n" \
    "v_exp_f32 v96, v96\nv_exp_f32 v97, v97\nv_mov_b32 v42, 1\nv_exp_f32 v98, v98\nv_exp_f32 v99, v99\nv_exp_f32 v100, v100\nv_exp_f32_e64 v101, v101\n" \
    "v_smfmac_f32_32x32x32_bf16 v[72:87], v[184:187], v[88:95], v43\nds_write_b128 v2, v[56:59] offset:0\nv_exp_f32 v102, v102\nv_exp_f32 v103, v103\n" \
    "v_exp_f32 v104, v104\nv_exp_f32 v105, v105\nv_smfmac_f32_32x32x32_bf16 v[72:87], v[188:191], v[96:103], v43\nds_write_b128 v2, v[60:63] offset:1024\n" \
    "v_exp_f32 v106, v106\nv_exp_f32 v107, v107\nv_exp_f32 v108, v108\nv_exp_f32 v109, v109\n" \
    "v_mfma_f32_32x32x16_bf16 v[120:135], v[160:163], v[50:53], v[120:135]\nds_write_b128 v2, v[64:67] offset:2048\nv_exp_f32 v110, v110\n" \
    "v_exp_f32 v111, v111\nv_exp_f32 v112, v112\nv_exp_f32 v113, v113\nv_mfma_f32_32x32x16_bf16 v[136:151], v[164:167], v[50:53], v[136:151]\n" \
    "ds_write2_b32 %[f0], v42, v42 offset0:0 offset1:4\nv_exp_f32 v114, v114\nv_exp_f32 v115, v115\nv_exp_f32 v116, v116\nv_exp_f32 v117, v117\n" \
    "v_smfmac_f32_32x32x32_bf16 v[72:87], v[192:195], v[104:111], v43\nv_exp_f32 v118, v118\nv_exp_f32 v119, v119\nv_exp_f32 v120, v120\n" \
    "v_exp_f32 v121, v121\nv_smfmac_f32_32x32x32_bf16 v[72:87], v[196:199], v[112:119], v43\nv_exp_f32 v122, v122\nv_exp_f32 v123, v123\n" \
    "v_exp_f32 v124, v124\nv_exp_f32 v125, v125\nv_mfma_f32_32x32x4_2b_bf16 v[88:119], v[252:253], v[54:55], 0\nv_exp_f32 v126, v126\n" \
    "v_exp_f32 v127, v127\nv_exp_f32 v128, v128\nv_exp_f32 v129, v129\nv_exp_f32 v130, v130\nv_exp_f32 v131, v131\nv_exp_f32 v132, v132\n" \
    "v_exp_f32 v133, v133\nv_smfmac_f32_32x32x32_bf16 v[72:87], v[200:203], v[120:127], v43\nv_exp_f32 v134, v134\nv_exp_f32 v135, v135\n" \
    "v_exp_f32 v136, v136\nv_exp_f32 v137, v137\nv_smfmac_f32_32x32x32_bf16 v[72:87], v[204:207], v[128:135], v43\nv_exp_f32 v138, v138\n" \
    "v_exp_f32 v139, v139\nv_exp_f32 v140, v140\nv_exp_f32 v141, v141\nv_mfma_f32_32x32x16_bf16 v[88:103], v[168:171], v[50:53], v[88:103]\n" \
    "v_exp_f32 v142, v142\nv_exp_f32 v143, v143\nv_exp_f32 v144, v144\nv_exp_f32 v145, v145\n" \
    "v_mfma_f32_32x32x16_bf16 v[104:119], v[172:175], v[50:53], v[104:119]\nv_exp_f32 v146, v146\nv_exp_f32 v147, v147\nv_exp_f32 v148, v148\n" \
    "v_exp_f32 v149, v149\nv_smfmac_f32_32x32x32_bf16 v[72:87], v[208:211], v[136:143], v43\nv_exp_f32 v150, v150\nv_exp_f32 v151, v151\n" \
    "v_exp_f32 v88, v88\nv_exp_f32 v89, v89\nv_smfmac_f32_32x32x32_bf16 v[72:87], v[212:215], v[144:151], v43\nds_read_b128 v[44:47], v7 offset:0\n" \
    "v_exp_f32 v90, v90\nv_exp_f32 v91, v91\nv_exp_f32 v92, v92\nv_exp_f32 v93, v93\nv_mfma_f32_32x32x4_2b_bf16 v[120:151], v[254:255], v[54:55], 0\n" \
    "v_exp_f32 v94, v94\nv_exp_f32 v95, v95\nv_exp_f32 v96, v96\nv_exp_f32 v97, v97\nv_exp_f32 v98, v98\nv_exp_f32 v99, v99\nv_exp_f32 v100, v100\n" \
    "v_exp_f32 v101, v101\nv_smfmac_f32_32x32x32_bf16 v[72:87], v[216:219], v[88:95], v43\nds_read_b64 v[48:49], v11 offset:0\nv_exp_f32 v102, v102\n" \
    "v_exp_f32 v103, v103\nv_exp_f32 v104, v104\nv_exp_f32 v105, v105\nv_smfmac_f32_32x32x32_bf16 v[72:87], v[220:223], v[96:103], v43\n" \
    "v_exp_f32 v106, v106\nv_exp_f32 v107, v107\nv_exp_f32 v108, v108\nv_exp_f32 v109, v109\n" \
    "v_mfma_f32_32x32x16_bf16 v[120:135], v[176:179], v[50:53], v[120:135]\nv_exp_f32 v110, v110\nv_exp_f32 v111, v111\nv_exp_f32 v112, v112\n" \
    "v_exp_f32 v113, v113\nv_mfma_f32_32x32x16_bf16 v[136:151], v[180:183], v[50:53], v[136:151]\nv_exp_f32 v114, v114\nv_exp_f32 v115, v115\n" \
    "v_exp_f32 v116, v116\nv_exp_f32 v117, v117\nv_smfmac_f32_32x32x32_bf16 v[72:87], v[224:227], v[104:111], v43\nv_exp_f32 v118, v118\n" \
    "v_exp_f32 v119, v119\nv_exp_f32 v120, v120\nv_exp_f32 v121, v121\nv_smfmac_f32_32x32x32_bf16 v[72:87], v[228:231], v[112:119], v43\n" \
    "ds_read_b32 v41, %[f0] offset:4\ns_waitcnt lgkmcnt(1)\nv_exp_f32 v122, v122\nv_exp_f32 v123, v123\nv_exp_f32 v124, v124\nv_exp_f32_e64 v125, v125\n" \
    "v_mfma_f32_32x32x4_2b_bf16 v[88:119], v[248:249], v[48:49], 0\nv_exp_f32 v126, v126\nv_exp_f32 v127, v127\nv_exp_f32 v128, v128\n" \
    "v_exp_f32 v129, v129\nv_exp_f32 v130, v130\nv_exp_f32 v131, v131\nv_exp_f32 v132, v132\nv_exp_f32 v133, v133\n" \
    "v_smfmac_f32_32x32x32_bf16 v[72:87], v[232:235], v[120:127], v43\nds_read_b128 v[56:59], v3 offset:0\nv_exp_f32 v134, v134\nv_exp_f32 v135, v135\n" \
    "v_exp_f32 v136, v136\nv_exp_f32 v137, v137\nv_smfmac_f32_32x32x32_bf16 v[72:87], v[236:239], v[128:135], v43\nds_read_b128 v[60:63], v3 offset:1024\n" \
    "s_waitcnt lgkmcnt(2)\nv_exp_f32 v138, v138\nv_exp_f32 v139, v139\nv_readfirstlane_b32 %[st], v41\nv_exp_f32 v140, v140\nv_exp_f32 v141, v141\n" \
    "v_mfma_f32_32x32x16_bf16 v[88:103], v[152:155], v[44:47], v[88:103]\nds_read_b128 v[64:67], v3 offset:2048\nv_exp_f32 v142, v142\n" \
    "v_exp_f32 v143, v143\nv_exp_f32 v144, v144\nv_exp_f32 v145, v145\nv_mfma_f32_32x32x16_bf16 v[104:119], v[156:159], v[44:47], v[104:119]\n" \
    "s_cmp_ge_u32 %[st], 1\ns_cbranch_scc0 .Lrt1_%=\n.Lok1_%=:\nv_exp_f32 v146, v146\nv_exp_f32 v147, v147\nv_exp_f32 v148, v148\nv_exp_f32 v149, v149\n" \
    "v_smfmac_f32_32x32x32_bf16 v[72:87], v[240:243], v[136:143], v43\nv_exp_f32 v150, v150\nv_exp_f32 v151, v151\n" \
    "v_exp_f32 v88, v88\nv_exp_f32 v89, v89\nv_smfmac_f32_32x32x32_bf16 v[72:87], v[244:247], v[144:151], v43\nv_exp_f32 v90, v90\nv_exp_f32 v91, v91\n" \
    "v_exp_f32 v92, v92\nv_exp_f32 v93, v93\nv_mfma_f32_32x32x4_2b_bf16 v[120:151], v[250:251], v[48:49], 0\ns_waitcnt lgkmcnt(0)\nv_exp_f32 v94, v94\n" \
    "v_exp_f32 v95, v95\nv_exp_f32 v96, v96\nv_exp_f32 v97, v97\nv_mov_b32 v42, 2\nv_exp_f32 v98, v98\nv_exp_f32 v99, v99\nv_exp_f32 v100, v100\n" \
    "v_exp_f32 v101, v101\nv_smfmac_f32_32x32x32_bf16 v[56:71], v[184:187], v[88:95], v43\nds_write_b128 v2, v[72:75] offset:12288\nv_exp_f32 v102, v102\n" \
    "v_exp_f32 v103, v103\nv_exp_f32 v104, v104\nv_exp_f32 v105, v105\nv_smfmac_f32_32x32x32_bf16 v[56:71], v[188:191], v[96:103], v43\n" \
    "ds_write_b128 v2, v[76:79] offset:13312\nv_exp_f32 v106, v106\nv_exp_f32 v107, v107\nv_exp_f32 v108, v108\nv_exp_f32 v109, v109\n" \
    "v_mfma_f32_32x32x16_bf16 v[120:135], v[160:163], v[44:47], v[120:135]\nds_write_b128 v2, v[80:83] offset:14336\nv_exp_f32 v110, v110\n" \
    "v_exp_f32 v111, v111\nv_exp_f32 v112, v112\nv_exp_f32 v113, v113\nv_mfma_f32_32x32x16_bf16 v[136:151], v[164:167], v[44:47], v[136:151]\n" \
    "ds_write2_b32 %[f0], v42, v42 offset0:0 offset1:4\nv_exp_f32 v114, v114\nv_exp_f32 v115, v115\nv_exp_f32 v116, v116\nv_exp_f32 v117, v117\n" \
    "v_smfmac_f32_32x32x32_bf16 v[56:71], v[192:195], v[104:111], v43\nv_exp_f32 v118, v118\nv_exp_f32 v119, v119\nv_exp_f32 v120, v120\n" \
    "v_exp_f32 v121, v121\nv_smfmac_f32_32x32x32_bf16 v[56:71], v[196:199], v[112:119], v43\nv_exp_f32 v122, v122\nv_exp_f32 v123, v123\n" \
    "v_exp_f32 v124, v124\nv_exp_f32 v125, v125\nv_mfma_f32_32x32x4_2b_bf16 v[88:119], v[252:253], v[48:49], 0\nv_exp_f32 v126, v126\n" \
    "v_exp_f32 v127, v127\nv_exp_f32 v128, v128\nv_exp_f32 v129, v129\nv_exp_f32 v130, v130\nv_exp_f32 v131, v131\nv_exp_f32 v132, v132\n" \
    "v_exp_f32 v133, v133\nv_smfmac_f32_32x32x32_bf16 v[56:71], v[200:203], v[120:127], v43\nv_exp_f32 v134, v134\nv_exp_f32 v135, v135\n" \
    "v_exp_f32 v136, v136\nv_exp_f32 v137, v137\nv_smfmac_f32_32x32x32_bf16 v[56:71], v[204:207], v[128:135], v43\nv_exp_f32 v138, v138\n" \
    "v_exp_f32 v139, v139\nv_exp_f32 v140, v140\nv_exp_f32 v141, v141\nv_mfma_f32_32x32x16_bf16 v[88:103], v[168:171], v[44:47], v[88:103]\n" \
    "v_exp_f32 v142, v142\nv_exp_f32 v143, v143\nv_exp_f32 v144, v144\nv_exp_f32 v145, v145\n" \
    "v_mfma_f32_32x32x16_bf16 v[104:119], v[172:175], v[44:47], v[104:119]\nv_exp_f32 v146, v146\nv_exp_f32 v147, v147\nv_exp_f32 v148, v148\n" \
    "v_exp_f32 v149, v149\nv_smfmac_f32_32x32x32_bf16 v[56:71], v[208:211], v[136:143], v43\nv_exp_f32 v150, v150\nv_exp_f32 v151, v151\n" \
    "v_exp_f32 v88, v88\nv_exp_f32 v89, v89\nv_smfmac_f32_32x32x32_bf16 v[56:71], v[212:215], v[144:151], v43\nds_read_b128 v[50:53], v9 offset:0\n" \
    "v_exp_f32 v90, v90\nv_exp_f32 v91, v91\nv_exp_f32 v92, v92\nv_exp_f32 v93, v93\nv_mfma_f32_32x32x4_2b_bf16 v[120:151], v[254:255], v[48:49], 0\n" \
    "v_exp_f32 v94, v94\nv_exp_f32 v95, v95\nv_exp_f32 v96, v96\nv_exp_f32 v97, v97\nv_exp_f32 v98, v98\nv_exp_f32 v99, v99\nv_exp_f32 v100, v100\n" \
    "v_exp_f32 v101, v101\nv_smfmac_f32_32x32x32_bf16 v[56:71], v[216:219], v[88:95], v43\nds_read_b64 v[54:55], v13 offset:0\nv_exp_f32 v102, v102\n" \
    "v_exp_f32 v103, v103\nv_exp_f32 v104, v104\nv_exp_f32 v105, v105\nv_smfmac_f32_32x32x32_bf16 v[56:71], v[220:223], v[96:103], v43\n" \
    "v_exp_f32 v106, v106\nv_exp_f32 v107, v107\nv_exp_f32 v108, v108\nv_exp_f32 v109, v109\n" \
    "v_mfma_f32_32x32x16_bf16 v[120:135], v[176:179], v[44:47], v[120:135]\nv_exp_f32 v110, v110\nv_exp_f32 v111, v111\nv_exp_f32 v112, v112\n" \
    "v_exp_f32 v113, v113\nv_mfma_f32_32x32x16_bf16 v[136:151], v[180:183], v[44:47], v[136:151]\nv_exp_f32 v114, v114\nv_exp_f32 v115, v115\n" \
    "v_exp_f32 v116, v116\nv_exp_f32 v117, v117\nv_smfmac_f32_32x32x32_bf16 v[56:71], v[224:227], v[104:111], v43\nv_exp_f32 v118, v118\n" \
    "v_exp_f32 v119, v119\nv_exp_f32 v120, v120\nv_exp_f32 v121, v121\nv_smfmac_f32_32x32x32_bf16 v[56:71], v[228:231], v[112:119], v43\n" \
    "ds_read_b32 v41, %[f0] offset:32\ns_waitcnt lgkmcnt(1)\nv_exp_f32 v122, v122\nv_exp_f32 v123, v123\nv_exp_f32 v124, v124\nv_exp_f32_e64 v125, v125\n" \
    "v_mfma_f32_32x32x4_2b_bf16 v[88:119], v[248:249], v[54:55], 0\nv_exp_f32 v126, v126\nv_exp_f32 v127, v127\nv_exp_f32 v128, v128\n" \
    "v_exp_f32 v129, v129\nv_exp_f32 v130, v130\nv_exp_f32 v131, v131\nv_exp_f32 v132, v132\nv_exp_f32 v133, v133\n" \
    "v_smfmac_f32_32x32x32_bf16 v[56:71], v[232:235], v[120:127], v43\nds_read_b128 v[72:75], v5 offset:24576\nv_exp_f32 v134, v134\nv_exp_f32 v135, v135\n" \
    "v_exp_f32 v136, v136\nv_exp_f32 v137, v137\nv_smfmac_f32_32x32x32_bf16 v[56:71], v[236:239], v[128:135], v43\nds_read_b128 v[76:79], v5 offset:25600\n" \
    "s_waitcnt lgkmcnt(2)\nv_exp_f32 v138, v138\nv_exp_f32 v139, v139\nv_readfirstlane_b32 %[st], v41\nv_exp_f32 v140, v140\nv_exp_f32 v141, v141\n" \
    "v_mfma_f32_32x32x16_bf16 v[88:103], v[152:155], v[50:53], v[88:103]\nds_read_b128 v[80:83], v5 offset:26624\nv_exp_f32 v142, v142\n" \
    "v_exp_f32 v143, v143\nv_exp_f32 v144, v144\nv_exp_f32 v145, v145\nv_mfma_f32_32x32x16_bf16 v[104:119], v[156:159], v[50:53], v[104:119]\n" \
    "s_cmp_ge_u32 %[st], 7\ns_cbranch_scc0 .Lrt2_%=\n.Lok2_%=:\nv_exp_f32 v146, v146\nv_exp_f32 v147, v147\nv_exp_f32 v148, v148\nv_exp_f32 v149, v149\n" \
    "v_smfmac_f32_32x32x32_bf16 v[56:71], v[240:243], v[136:143], v43\nv_exp_f32 v150, v150\nv_exp_f32 v151, v151\n" \
    "v_exp_f32 v88, v88\nv_exp_f32 v89, v89\nv_smfmac_f32_32x32x32_bf16 v[56:71], v[244:247], v[144:151], v43\nv_exp_f32 v90, v90\nv_exp_f32 v91, v91\n" \
    "v_exp_f32 v92, v92\nv_exp_f32 v93, v93\nv_mfma_f32_32x32x4_2b_bf16 v[120:151], v[250:251], v[54:55], 0\ns_waitcnt lgkmcnt(0)\nv_exp_f32 v94, v94\n" \
    "v_exp_f32 v95, v95\nv_exp_f32 v96, v96\nv_exp_f32 v97, v97\nv_mov_b32 v42, 3\nv_exp_f32 v98, v98\nv_exp_f32 v99, v99\nv_exp_f32 v100, v100\n" \
    "v_exp_f32 v101, v101\nv_smfmac_f32_32x32x32_bf16 v[72:87], v[184:187], v[88:95], v43\nds_write_b128 v3, v[56:59] offset:0\nv_exp_f32 v102, v102\n" \
    "v_exp_f32 v103, v103\nv_exp_f32 v104, v104\nv_exp_f32 v105, v105\nv_smfmac_f32_32x32x32_bf16 v[72:87], v[188:191], v[96:103], v43\n" \
    "ds_write_b128 v3, v[60:63] offset:1024\nv_exp_f32 v106, v106\nv_exp_f32 v107, v107\nv_exp_f32 v108, v108\nv_exp_f32 v109, v109\n" \
    "v_mfma_f32_32x32x16_bf16 v[120:135], v[160:163], v[50:53], v[120:135]\nds_write_b128 v3, v[64:67] offset:2048\nv_exp_f32 v110, v110\n" \
    "v_exp_f32 v111, v111\nv_exp_f32 v112, v112\nv_exp_f32 v113, v113\nv_mfma_f32_32x32x16_bf16 v[136:151], v[164:167], v[50:53], v[136:151]\n" \
    "ds_write2_b32 %[f0], v42, v42 offset0:0 offset1:4\nv_exp_f32 v114, v114\nv_exp_f32 v115, v115\nv_exp_f32 v116, v116\nv_exp_f32 v117, v117\n" \
    "v_smfmac_f32_32x32x32_bf16 v[72:87], v[192:195], v[104:111], v43\nv_exp_f32 v118, v118\nv_exp_f32 v119, v119\nv_exp_f32 v120, v120\n" \
    "v_exp_f32 v121, v121\nv_smfmac_f32_32x32x32_bf16 v[72:87], v[196:199], v[112:119], v43\nv_exp_f32 v122, v122\nv_exp_f32 v123, v123\n" \
    "v_exp_f32 v124, v124\nv_exp_f32 v125, v125\nv_mfma_f32_32x32x4_2b_bf16 v[88:119], v[252:253], v[54:55], 0\nv_exp_f32 v126, v126\n" \
    "v_exp_f32 v127, v127\nv_exp_f32 v128, v128\nv_exp_f32 v129, v129\nv_exp_f32 v130, v130\nv_exp_f32 v131, v131\nv_exp_f32 v132, v132\n" \
    "v_exp_f32 v133, v133\nv_smfmac_f32_32x32x32_bf16 v[72:87], v[200:203], v[120:127], v43\nv_exp_f32 v134, v134\nv_exp_f32 v135, v135\n" \
    "v_exp_f32 v136, v136\nv_exp_f32 v137, v137\nv_smfmac_f32_32x32x32_bf16 v[72:87], v[204:207], v[128:135], v43\nv_exp_f32 v138, v138\n" \
    "v_exp_f32 v139, v139\nv_exp_f32 v140, v140\nv_exp_f32 v141, v141\nv_mfma_f32_32x32x16_bf16 v[88:103], v[168:171], v[50:53], v[88:103]\n" \
    "v_exp_f32 v142, v142\nv_exp_f32 v143, v143\nv_exp_f32 v144, v144\nv_exp_f32 v145, v145\n" \
    "v_mfma_f32_32x32x16_bf16 v[104:119], v[172:175], v[50:53], v[104:119]\nv_exp_f32 v146, v146\nv_exp_f32 v147, v147\nv_exp_f32 v148, v148\n" \
    "v_exp_f32 v149, v149\nv_smfmac_f32_32x32x32_bf16 v[72:87], v[208:211], v[136:143], v43\nv_exp_f32 v150, v150\nv_exp_f32 v151, v151\n" \
    "v_exp_f32 v88, v88\nv_exp_f32 v89, v89\nv_smfmac_f32_32x32x32_bf16 v[72:87], v[212:215], v[144:151], v43\nds_read_b128 v[44:47], v8 offset:0\n" \
    "v_exp_f32 v90, v90\nv_exp_f32 v91, v91\nv_exp_f32 v92, v92\nv_exp_f32 v93, v93\nv_mfma_f32_32x32x4_2b_bf16 v[120:151], v[254:255], v[54:55], 0\n" \
    "v_exp_f32 v94, v94\nv_exp_f32 v95, v95\nv_exp_f32 v96, v96\nv_exp_f32 v97, v97\nv_exp_f32 v98, v98\nv_exp_f32 v99, v99\nv_exp_f32 v100, v100\n" \
    "v_exp_f32 v101, v101\nv_smfmac_f32_32x32x32_bf16 v[72:87], v[216:219], v[88:95], v43\nds_read_b64 v[48:49], v12 offset:0\nv_exp_f32 v102, v102\n" \
    "v_exp_f32 v103, v103\nv_exp_f32 v104, v104\nv_exp_f32 v105, v105\nv_smfmac_f32_32x32x32_bf16 v[72:87], v[220:223], v[96:103], v43\n" \
    "v_exp_f32 v106, v106\nv_exp_f32 v107, v107\nv_exp_f32 v108, v108\nv_exp_f32 v109, v109\n" \
    "v_mfma_f32_32x32x16_bf16 v[120:135], v[176:179], v[50:53], v[120:135]\nv_exp_f32 v110, v110\nv_exp_f32 v111, v111\nv_exp_f32 v112, v112\n" \
    "v_exp_f32 v113, v113\nv_mfma_f32_32x32x16_bf16 v[136:151], v[180:183], v[50:53], v[136:151]\nv_exp_f32 v114, v114\nv_exp_f32 v115, v115\n" \
    "v_exp_f32 v116, v116\nv_exp_f32 v117, v117\nv_smfmac_f32_32x32x32_bf16 v[72:87], v[224:227], v[104:111], v43\nv_exp_f32 v118, v118\n" \
    "v_exp_f32 v119, v119\nv_exp_f32 v120, v120\nv_exp_f32 v121, v121\nv_smfmac_f32_32x32x32_bf16 v[72:87], v[228:231], v[112:119], v43\n" \
    "ds_read_b32 v41, %[f0] offset:4\ns_waitcnt lgkmcnt(1)\nv_exp_f32 v122, v122\nv_exp_f32 v123, v123\nv_exp_f32 v124, v124\nv_exp_f32_e64 v125, v125\n" \
    "v_mfma_f32_32x32x4_2b_bf16 v[88:119], v[248:249], v[48:49], 0\nv_exp_f32 v126, v126\nv_exp_f32 v127, v127\nv_exp_f32 v128, v128\n" \
    "v_exp_f32 v129, v129\nv_exp_f32 v130, v130\nv_exp_f32 v131, v131\nv_exp_f32 v132, v132\nv_exp_f32 v133, v133\n" \
    "v_smfmac_f32_32x32x32_bf16 v[72:87], v[232:235], v[120:127], v43\nds_read_b128 v[56:59], v4 offset:0\nv_exp_f32 v134, v134\nv_exp_f32 v135, v135\n" \
    "v_exp_f32 v136, v136\nv_exp_f32 v137, v137\nv_smfmac_f32_32x32x32_bf16 v[72:87], v[236:239], v[128:135], v43\nds_read_b128 v[60:63], v4 offset:1024\n" \
    "s_waitcnt lgkmcnt(2)\nv_exp_f32 v138, v138\nv_exp_f32 v139, v139\nv_readfirstlane_b32 %[st], v41\nv_exp_f32 v140, v140\nv_exp_f32 v141, v141\n" \
    "v_mfma_f32_32x32x16_bf16 v[88:103], v[152:155], v[44:47], v[88:103]\nds_read_b128 v[64:67], v4 offset:2048\nv_exp_f32 v142, v142\n" \
    "v_exp_f32 v143, v143\nv_exp_f32 v144, v144\nv_exp_f32 v145, v145\nv_mfma_f32_32x32x16_bf16 v[104:119], v[156:159], v[44:47], v[104:119]\n" \
    "s_cmp_ge_u32 %[st], 3\ns_cbranch_scc0 .Lrt3_%=\n.Lok3_%=:\nv_exp_f32 v146, v146\nv_exp_f32 v147, v147\nv_exp_f32 v148, v148\nv_exp_f32 v149, v149\n" \
    "v_smfmac_f32_32x32x32_bf16 v[72:87], v[240:243], v[136:143], v43\nv_exp_f32 v150, v150\nv_exp_f32 v151, v151\n" \
    "v_exp_f32 v88, v88\nv_exp_f32 v89, v89\nv_smfmac_f32_32x32x32_bf16 v[72:87], v[244:247], v[144:151], v43\nv_exp_f32 v90, v90\nv_exp_f32 v91, v91\n" \
    "v_exp_f32 v92, v92\nv_exp_f32 v93, v93\nv_mfma_f32_32x32x4_2b_bf16 v[120:151], v[250:251], v[48:49], 0\ns_waitcnt lgkmcnt(0)\nv_exp_f32 v94, v94\n" \
    "v_exp_f32 v95, v95\nv_exp_f32 v96, v96\nv_exp_f32 v97, v97\nv_mov_b32 v42, 4\nv_exp_f32 v98, v98\nv_exp_f32 v99, v99\nv_exp_f32 v100, v100\n" \
    "v_exp_f32 v101, v101\nv_smfmac_f32_32x32x32_bf16 v[56:71], v[184:187], v[88:95], v43\nds_write_b128 v5, v[72:75] offset:24576\nv_exp_f32 v102, v102\n" \
    "v_exp_f32 v103, v103\nv_exp_f32 v104, v104\nv_exp_f32 v105, v105\nv_smfmac_f32_32x32x32_bf16 v[56:71], v[188:191], v[96:103], v43\n" \
    "ds_write_b128 v5, v[76:79] offset:25600\nv_exp_f32 v106, v106\nv_exp_f32 v107, v107\nv_exp_f32 v108, v108\nv_exp_f32 v109, v109\n" \
    "v_mfma_f32_32x32x16_bf16 v[120:135], v[160:163], v[44:47], v[120:135]\nds_write_b128 v5, v[80:83] offset:26624\nv_exp_f32 v110, v110\n" \
    "v_exp_f32 v111, v111\nv_exp_f32 v112, v112\nv_exp_f32 v113, v113\nv_mfma_f32_32x32x16_bf16 v[136:151], v[164:167], v[44:47], v[136:151]\n" \
    "ds_write2_b32 %[f0], v42, v42 offset0:0 offset1:4\nv_exp_f32 v114, v114\nv_exp_f32 v115, v115\nv_exp_f32 v116, v116\nv_exp_f32 v117, v117\n" \
    "v_smfmac_f32_32x32x32_bf16 v[56:71], v[192:195], v[104:111], v43\nv_exp_f32 v118, v118\nv_exp_f32 v119, v119\nv_exp_f32 v120, v120\n" \
    "v_exp_f32 v121, v121\nv_smfmac_f32_32x32x32_bf16 v[56:71], v[196:199], v[112:119], v43\nv_exp_f32 v122, v122\nv_exp_f32 v123, v123\n" \
    "v_exp_f32 v124, v124\nv_exp_f32 v125, v125\nv_mfma_f32_32x32x4_2b_bf16 v[88:119], v[252:253], v[48:49], 0\nv_exp_f32 v126, v126\n" \
    "v_exp_f32 v127, v127\nv_exp_f32 v128, v128\nv_exp_f32 v129, v129\nv_exp_f32 v130, v130\nv_exp_f32 v131, v131\nv_exp_f32 v132, v132\n" \
    "v_exp_f32 v133, v133\nv_smfmac_f32_32x32x32_bf16 v[56:71], v[200:203], v[120:127], v43\nv_exp_f32 v134, v134\nv_exp_f32 v135, v135\n" \
    "v_exp_f32 v136, v136\nv_exp_f32 v137, v137\nv_smfmac_f32_32x32x32_bf16 v[56:71], v[204:207], v[128:135], v43\nv_exp_f32 v138, v138\n" \
    "v_exp_f32 v139, v139\nv_exp_f32 v140, v140\nv_exp_f32 v141, v141\nv_mfma_f32_32x32x16_bf16 v[88:103], v[168:171], v[44:47], v[88:103]\n" \
    "v_exp_f32 v142, v142\nv_exp_f32 v143, v143\nv_exp_f32 v144, v144\nv_exp_f32 v145, v145\n" \
    "v_mfma_f32_32x32x16_bf16 v[104:119], v[172:175], v[44:47], v[104:119]\nv_exp_f32 v146, v146\nv_exp_f32 v147, v147\nv_exp_f32 v148, v148\n" \
    "v_exp_f32 v149, v149\nv_smfmac_f32_32x32x32_bf16 v[56:71], v[208:211], v[136:143], v43\nv_exp_f32 v150, v150\nv_exp_f32 v151, v151\n" \
    "v_exp_f32 v88, v88\nv_exp_f32 v89, v89\nv_smfmac_f32_32x32x32_bf16 v[56:71], v[212:215], v[144:151], v43\nds_read_b128 v[50:53], v7 offset:1280\n" \
    "v_exp_f32 v90, v90\nv_exp_f32 v91, v91\nv_exp_f32 v92, v92\nv_exp_f32 v93, v93\nv_mfma_f32_32x32x4_2b_bf16 v[120:151], v[254:255], v[48:49], 0\n" \
    "v_exp_f32 v94, v94\nv_exp_f32 v95, v95\nv_exp_f32 v96, v96\nv_exp_f32 v97, v97\nv_exp_f32 v98, v98\nv_exp_f32 v99, v99\nv_exp_f32 v100, v100\n" \
    "v_exp_f32 v101, v101\nv_smfmac_f32_32x32x32_bf16 v[56:71], v[216:219], v[88:95], v43\nds_read_b64 v[54:55], v11 offset:1280\nv_exp_f32 v102, v102\n" \
    "v_exp_f32 v103, v103\nv_exp_f32 v104, v104\nv_exp_f32 v105, v105\nv_smfmac_f32_32x32x32_bf16 v[56:71], v[220:223], v[96:103], v43\n" \
    "v_exp_f32 v106, v106\nv_exp_f32 v107, v107\nv_exp_f32 v108, v108\nv_exp_f32 v109, v109\n" \
    "v_mfma_f32_32x32x16_bf16 v[120:135], v[176:179], v[44:47], v[120:135]\nv_exp_f32 v110, v110\nv_exp_f32 v111, v111\nv_exp_f32 v112, v112\n" \
    "v_exp_f32 v113, v113\nv_mfma_f32_32x32x16_bf16 v[136:151], v[180:183], v[44:47], v[136:151]\nv_exp_f32 v114, v114\nv_exp_f32 v115, v115\n" \
    "v_exp_f32 v116, v116\nv_exp_f32 v117, v117\nv_smfmac_f32_32x32x32_bf16 v[56:71], v[224:227], v[104:111], v43\nv_exp_f32 v118, v118\n" \
    "v_exp_f32 v119, v119\nv_exp_f32 v120, v120\nv_exp_f32 v121, v121\nv_smfmac_f32_32x32x32_bf16 v[56:71], v[228:231], v[112:119], v43\n" \
    "ds_read_b32 v41, %[f0] offset:4\ns_waitcnt lgkmcnt(1)\nv_exp_f32 v122, v122\nv_exp_f32 v123, v123\nv_exp_f32 v124, v124\nv_exp_f32_e64 v125, v125\n" \
    "v_mfma_f32_32x32x4_2b_bf16 v[88:119], v[248:249], v[54:55], 0\nv_exp_f32 v126, v126\nv_exp_f32 v127, v127\nv_exp_f32 v128, v128\n" \
    "v_exp_f32 v129, v129\nv_exp_f32 v130, v130\nv_exp_f32 v131, v131\nv_exp_f32 v132, v132\nv_exp_f32 v133, v133\n" \
    "v_smfmac_f32_32x32x32_bf16 v[56:71], v[232:235], v[120:127], v43\nds_read_b128 v[72:75], v3 offset:12288\nv_exp_f32 v134, v134\nv_exp_f32 v135, v135\n" \
    "v_exp_f32 v136, v136\nv_exp_f32 v137, v137\nv_smfmac_f32_32x32x32_bf16 v[56:71], v[236:239], v[128:135], v43\nds_read_b128 v[76:79], v3 offset:13312\n" \
    "s_waitcnt lgkmcnt(2)\nv_exp_f32 v138, v138\nv_exp_f32 v139, v139\nv_readfirstlane_b32 %[st], v41\nv_exp_f32 v140, v140\nv_exp_f32 v141, v141\n" \
    "v_mfma_f32_32x32x16_bf16 v[88:103], v[152:155], v[50:53], v[88:103]\nds_read_b128 v[80:83], v3 offset:14336\nv_exp_f32 v142, v142\n" \
    "v_exp_f32 v143, v143\nv_exp_f32 v144, v144\nv_exp_f32 v145, v145\nv_mfma_f32_32x32x16_bf16 v[104:119], v[156:159], v[50:53], v[104:119]\n" \
    "s_cmp_ge_u32 %[st], 2\ns_cbranch_scc0 .Lrt4_%=\n.Lok4_%=:\nv_exp_f32 v146, v146\nv_exp_f32 v147, v147\nv_exp_f32 v148, v148\nv_exp_f32 v149, v149\n" \
    "v_smfmac_f32_32x32x32_bf16 v[56:71], v[240:243], v[136:143], v43\nv_exp_f32 v150, v150\nv_exp_f32 v151, v151\n" \
    "v_exp_f32 v88, v88\nv_exp_f32 v89, v89\nv_smfmac_f32_32x32x32_bf16 v[56:71], v[244:247], v[144:151], v43\nv_exp_f32 v90, v90\nv_exp_f32 v91, v91\n" \
    "v_exp_f32 v92, v92\nv_exp_f32 v93, v93\nv_mfma_f32_32x32x4_2b_bf16 v[120:151], v[250:251], v[54:55], 0\ns_waitcnt lgkmcnt(0)\nv_exp_f32 v94, v94\n" \
    "v_exp_f32 v95, v95\nv_exp_f32 v96, v96\nv_exp_f32 v97, v97\nv_mov_b32 v42, 5\nv_exp_f32 v98, v98\nv_exp_f32 v99, v99\nv_exp_f32 v100, v100\n" \
    "v_exp_f32 v101, v101\nv_smfmac_f32_32x32x32_bf16 v[72:87], v[184:187], v[88:95], v43\nds_write_b128 v4, v[56:59] offset:0\nv_exp_f32 v102, v102\n" \
    "v_exp_f32 v103, v103\nv_exp_f32 v104, v104\nv_exp_f32 v105, v105\nv_smfmac_f32_32x32x32_bf16 v[72:87], v[188:191], v[96:103], v43\n" \
    "ds_write_b128 v4, v[60:63] offset:1024\nv_exp_f32 v106, v106\nv_exp_f32 v107, v107\nv_exp_f32 v108, v108\nv_exp_f32 v109, v109\n" \
    "v_mfma_f32_32x32x16_bf16 v[120:135], v[160:163], v[50:53], v[120:135]\nds_write_b128 v4, v[64:67] offset:2048\nv_exp_f32 v110, v110\n" \
    "v_exp_f32 v111, v111\nv_exp_f32 v112, v112\nv_exp_f32 v113, v113\nv_mfma_f32_32x32x16_bf16 v[136:151], v[164:167], v[50:53], v[136:151]\n" \
    "ds_write2_b32 %[f0], v42, v42 offset0:0 offset1:4\nv_exp_f32 v114, v114\nv_exp_f32 v115, v115\nv_exp_f32 v116, v116\nv_exp_f32 v117, v117\n" \
    "v_smfmac_f32_32x32x32_bf16 v[72:87], v[192:195], v[104:111], v43\nv_exp_f32 v118, v118\nv_exp_f32 v119, v119\nv_exp_f32 v120, v120\n" \
    "v_exp_f32 v121, v121\nv_smfmac_f32_32x32x32_bf16 v[72:87], v[196:199], v[112:119], v43\nv_exp_f32 v122, v122\nv_exp_f32 v123, v123\n" \
    "v_exp_f32 v124, v124\nv_exp_f32 v125, v125\nv_mfma_f32_32x32x4_2b_bf16 v[88:119], v[252:253], v[54:55], 0\nv_exp_f32 v126, v126\n" \
    "v_exp_f32 v127, v127\nv_exp_f32 v128, v128\nv_exp_f32 v129, v129\nv_exp_f32 v130, v130\nv_exp_f32 v131, v131\nv_exp_f32 v132, v132\n" \
    "v_exp_f32 v133, v133\nv_smfmac_f32_32x32x32_bf16 v[72:87], v[200:203], v[120:127], v43\nv_exp_f32 v134, v134\nv_exp_f32 v135, v135\n" \
    "v_exp_f32 v136, v136\nv_exp_f32 v137, v137\nv_smfmac_f32_32x32x32_bf16 v[72:87], v[204:207], v[128:135], v43\nv_exp_f32 v138, v138\n" \
    "v_exp_f32 v139, v139\nv_exp_f32 v140, v140\nv_exp_f32 v141, v141\nv_mfma_f32_32x32x16_bf16 v[88:103], v[168:171], v[50:53], v[88:103]\n" \
    "v_exp_f32 v142, v142\nv_exp_f32 v143, v143\nv_exp_f32 v144, v144\nv_exp_f32 v145, v145\n" \
    "v_mfma_f32_32x32x16_bf16 v[104:119], v[172:175], v[50:53], v[104:119]\nv_exp_f32 v146, v146\nv_exp_f32 v147, v147\nv_exp_f32 v148, v148\n" \
    "v_exp_f32 v149, v149\nv_smfmac_f32_32x32x32_bf16 v[72:87], v[208:211], v[136:143], v43\nv_exp_f32 v150, v150\nv_exp_f32 v151, v151\n" \
    "v_exp_f32 v88, v88\nv_exp_f32 v89, v89\nv_smfmac_f32_32x32x32_bf16 v[72:87], v[212:215], v[144:151], v43\nds_read_b128 v[44:47], v9 offset:1280\n" \
    "v_exp_f32 v90, v90\nv_exp_f32 v91, v91\nv_exp_f32 v92, v92\nv_exp_f32 v93, v93\nv_mfma_f32_32x32x4_2b_bf16 v[120:151], v[254:255], v[54:55], 0\n" \
    "v_exp_f32 v94, v94\nv_exp_f32 v95, v95\nv_exp_f32 v96, v96\nv_exp_f32 v97, v97\nv_exp_f32 v98, v98\nv_exp_f32 v99, v99\nv_exp_f32 v100, v100\n" \
    "v_exp_f32 v101, v101\nv_smfmac_f32_32x32x32_bf16 v[72:87], v[216:219], v[88:95], v43\nds_read_b64 v[48:49], v13 offset:1280\nv_exp_f32 v102, v102\n" \
    "v_exp_f32 v103, v103\nv_exp_f32 v104, v104\nv_exp_f32 v105, v105\nv_smfmac_f32_32x32x32_bf16 v[72:87], v[220:223], v[96:103], v43\n" \
    "v_exp_f32 v106, v106\nv_exp_f32 v107, v107\nv_exp_f32 v108, v108\nv_exp_f32 v109, v109\n" \
    "v_mfma_f32_32x32x16_bf16 v[120:135], v[176:179], v[50:53], v[120:135]\nv_exp_f32 v110, v110\nv_exp_f32 v111, v111\nv_exp_f32 v112, v112\n" \
    "v_exp_f32 v113, v113\nv_mfma_f32_32x32x16_bf16 v[136:151], v[180:183], v[50:53], v[136:151]\nv_exp_f32 v114, v114\nv_exp_f32 v115, v115\n" \
    "v_exp_f32 v116, v116\nv_exp_f32 v117, v117\nv_smfmac_f32_32x32x32_bf16 v[72:87], v[224:227], v[104:111], v43\nv_exp_f32 v118, v118\n" \
    "v_exp_f32 v119, v119\nv_exp_f32 v120, v120\nv_exp_f32 v121, v121\nv_smfmac_f32_32x32x32_bf16 v[72:87], v[228:231], v[112:119], v43\n" \
    "ds_read_b32 v41, %[f0] offset:32\ns_waitcnt lgkmcnt(1)\nv_exp_f32 v122, v122\nv_exp_f32 v123, v123\nv_exp_f32 v124, v124\nv_exp_f32_e64 v125, v125\n" \
    "v_mfma_f32_32x32x4_2b_bf16 v[88:119], v[248:249], v[48:49], 0\nv_exp_f32 v126, v126\nv_exp_f32 v127, v127\nv_exp_f32 v128, v128\n" \
    "v_exp_f32 v129, v129\nv_exp_f32 v130, v130\nv_exp_f32 v131, v131\nv_exp_f32 v132, v132\nv_exp_f32 v133, v133\n" \
    "v_smfmac_f32_32x32x32_bf16 v[72:87], v[232:235], v[120:127], v43\nds_read_b128 v[56:59], v5 offset:36864\nv_exp_f32 v134, v134\nv_exp_f32 v135, v135\n" \
    "v_exp_f32 v136, v136\nv_exp_f32 v137, v137\nv_smfmac_f32_32x32x32_bf16 v[72:87], v[236:239], v[128:135], v43\nds_read_b128 v[60:63], v5 offset:37888\n" \
    "s_waitcnt lgkmcnt(2)\nv_exp_f32 v138, v138\nv_exp_f32 v139, v139\nv_readfirstlane_b32 %[st], v41\nv_exp_f32 v140, v140\nv_exp_f32 v141, v141\n" \
    "v_mfma_f32_32x32x16_bf16 v[88:103], v[152:155], v[44:47], v[88:103]\nds_read_b128 v[64:67], v5 offset:38912\nv_exp_f32 v142, v142\n" \
    "v_exp_f32 v143, v143\nv_exp_f32 v144, v144\nv_exp_f32 v145, v145\nv_mfma_f32_32x32x16_bf16 v[104:119], v[156:159], v[44:47], v[104:119]\n" \
    "s_cmp_ge_u32 %[st], 8\ns_cbranch_scc0 .Lrt5_%=\n.Lok5_%=:\nv_exp_f32 v146, v146\nv_exp_f32 v147, v147\nv_exp_f32 v148, v148\nv_exp_f32 v149, v149\n" \
    "v_smfmac_f32_32x32x32_bf16 v[72:87], v[240:243], v[136:143], v43\nv_exp_f32 v150, v150\nv_exp_f32 v151, v151\n" \
    "v_exp_f32 v88, v88\nv_exp_f32 v89, v89\nv_smfmac_f32_32x32x32_bf16 v[72:87], v[244:247], v[144:151], v43\nv_exp_f32 v90, v90\nv_exp_f32 v91, v91\n" \
    "v_exp_f32 v92, v92\nv_exp_f32 v93, v93\nv_mfma_f32_32x32x4_2b_bf16 v[120:151], v[250:251], v[48:49], 0\ns_waitcnt lgkmcnt(0)\nv_exp_f32 v94, v94\n" \
    "v_exp_f32 v95, v95\nv_exp_f32 v96, v96\nv_exp_f32 v97, v97\nv_mov_b32 v42, 6\nv_exp_f32 v98, v98\nv_exp_f32 v99, v99\nv_exp_f32 v100, v100\n" \
    "v_exp_f32 v101, v101\nv_smfmac_f32_32x32x32_bf16 v[56:71], v[184:187], v[88:95], v43\nds_write_b128 v3, v[72:75] offset:12288\nv_exp_f32 v102, v102\n" \
    "v_exp_f32 v103, v103\nv_exp_f32 v104, v104\nv_exp_f32 v105, v105\nv_smfmac_f32_32x32x32_bf16 v[56:71], v[188:191], v[96:103], v43\n" \
    "ds_write_b128 v3, v[76:79] offset:13312\nv_exp_f32 v106, v106\nv_exp_f32 v107, v107\nv_exp_f32 v108, v108\nv_exp_f32 v109, v109\n" \
    "v_mfma_f32_32x32x16_bf16 v[120:135], v[160:163], v[44:47], v[120:135]\nds_write_b128 v3, v[80:83] offset:14336\nv_exp_f32 v110, v110\n" \
    "v_exp_f32 v111, v111\nv_exp_f32 v112, v112\nv_exp_f32 v113, v113\nv_mfma_f32_32x32x16_bf16 v[136:151], v[164:167], v[44:47], v[136:151]\n" \
    "ds_write2_b32 %[f0], v42, v42 offset0:0 offset1:4\nv_exp_f32 v114, v114\nv_exp_f32 v115, v115\nv_exp_f32 v116, v116\nv_exp_f32 v117, v117\n" \
    "v_smfmac_f32_32x32x32_bf16 v[56:71], v[192:195], v[104:111], v43\nv_exp_f32 v118, v118\nv_exp_f32 v119, v119\nv_exp_f32 v120, v120\n" \
    "v_exp_f32 v121, v121\nv_smfmac_f32_32x32x32_bf16 v[56:71], v[196:199], v[112:119], v43\nv_exp_f32 v122, v122\nv_exp_f32 v123, v123\n" \
    "v_exp_f32 v124, v124\nv_exp_f32 v125, v125\nv_mfma_f32_32x32x4_2b_bf16 v[88:119], v[252:253], v[48:49], 0\nv_exp_f32 v126, v126\n" \
    "v_exp_f32 v127, v127\nv_exp_f32 v128, v128\nv_exp_f32 v129, v129\nv_exp_f32 v130, v130\nv_exp_f32 v131, v131\nv_exp_f32 v132, v132\n" \
    "v_exp_f32 v133, v133\nv_smfmac_f32_32x32x32_bf16 v[56:71], v[200:203], v[120:127], v43\nv_exp_f32 v134, v134\nv_exp_f32 v135, v135\n" \
    "v_exp_f32 v136, v136\nv_exp_f32 v137, v137\nv_smfmac_f32_32x32x32_bf16 v[56:71], v[204:207], v[128:135], v43\nv_exp_f32 v138, v138\n" \
    "v_exp_f32 v139, v139\nv_exp_f32 v140, v140\nv_exp_f32 v141, v141\nv_mfma_f32_32x32x16_bf16 v[88:103], v[168:171], v[44:47], v[88:103]\n" \
    "v_exp_f32 v142, v142\nv_exp_f32 v143, v143\nv_exp_f32 v144, v144\nv_exp_f32 v145, v145\n" \
    "v_mfma_f32_32x32x16_bf16 v[104:119], v[172:175], v[44:47], v[104:119]\nv_exp_f32 v146, v146\nv_exp_f32 v147, v147\nv_exp_f32 v148, v148\n" \
    "v_exp_f32 v149, v149\nv_smfmac_f32_32x32x32_bf16 v[56:71], v[208:211], v[136:143], v43\nv_exp_f32 v150, v150\nv_exp_f32 v151, v151\n" \
    "v_exp_f32 v88, v88\nv_exp_f32 v89, v89\nv_smfmac_f32_32x32x32_bf16 v[56:71], v[212:215], v[144:151], v43\nds_read_b128 v[50:53], v8 offset:1280\n" \
    "v_exp_f32 v90, v90\nv_exp_f32 v91, v91\nv_exp_f32 v92, v92\nv_exp_f32 v93, v93\nv_mfma_f32_32x32x4_2b_bf16 v[120:151], v[254:255], v[48:49], 0\n" \
    "v_exp_f32 v94, v94\nv_exp_f32 v95, v95\nv_exp_f32 v96, v96\nv_exp_f32 v97, v97\nv_exp_f32 v98, v98\nv_exp_f32 v99, v99\nv_exp_f32 v100, v100\n" \
    "v_exp_f32 v101, v101\nv_smfmac_f32_32x32x32_bf16 v[56:71], v[216:219], v[88:95], v43\nds_read_b64 v[54:55], v12 offset:1280\nv_exp_f32 v102, v102\n" \
    "v_exp_f32 v103, v103\nv_exp_f32 v104, v104\nv_exp_f32 v105, v105\nv_smfmac_f32_32x32x32_bf16 v[56:71], v[220:223], v[96:103], v43\n" \
    "v_exp_f32 v106, v106\nv_exp_f32 v107, v107\nv_exp_f32 v108, v108\nv_exp_f32 v109, v109\n" \
    "v_mfma_f32_32x32x16_bf16 v[120:135], v[176:179], v[44:47], v[120:135]\nv_exp_f32 v110, v110\nv_exp_f32 v111, v111\nv_exp_f32 v112, v112\n" \
    "v_exp_f32 v113, v113\nv_mfma_f32_32x32x16_bf16 v[136:151], v[180:183], v[44:47], v[136:151]\nv_exp_f32 v114, v114\nv_exp_f32 v115, v115\n" \
    "v_exp_f32 v116, v116\nv_exp_f32 v117, v117\nv_smfmac_f32_32x32x32_bf16 v[56:71], v[224:227], v[104:111], v43\nv_exp_f32 v118, v118\n" \
    "v_exp_f32 v119, v119\nv_exp_f32 v120, v120\nv_exp_f32 v121, v121\nv_smfmac_f32_32x32x32_bf16 v[56:71], v[228:231], v[112:119], v43\n" \
    "ds_read_b32 v41, %[f0] offset:4\ns_waitcnt lgkmcnt(1)\nv_exp_f32 v122, v122\nv_exp_f32 v123, v123\nv_exp_f32 v124, v124\nv_exp_f32_e64 v125, v125\n" \
    "v_mfma_f32_32x32x4_2b_bf16 v[88:119], v[248:249], v[54:55], 0\nv_exp_f32 v126, v126\nv_exp_f32 v127, v127\nv_exp_f32 v128, v128\n" \
    "v_exp_f32 v129, v129\nv_exp_f32 v130, v130\nv_exp_f32 v131, v131\nv_exp_f32 v132, v132\nv_exp_f32 v133, v133\n" \
    "v_smfmac_f32_32x32x32_bf16 v[56:71], v[232:235], v[120:127], v43\nds_read_b128 v[72:75], v4 offset:12288\nv_exp_f32 v134, v134\nv_exp_f32 v135, v135\n" \
    "v_exp_f32 v136, v136\nv_exp_f32 v137, v137\nv_smfmac_f32_32x32x32_bf16 v[56:71], v[236:239], v[128:135], v43\nds_read_b128 v[76:79], v4 offset:13312\n" \
    "s_waitcnt lgkmcnt(2)\nv_exp_f32 v138, v138\nv_exp_f32 v139, v139\nv_readfirstlane_b32 %[st], v41\nv_exp_f32 v140, v140\nv_exp_f32 v141, v141\n" \
    "v_mfma_f32_32x32x16_bf16 v[88:103], v[152:155], v[50:53], v[88:103]\nds_read_b128 v[80:83], v4 offset:14336\nv_exp_f32 v142, v142\n" \
    "v_exp_f32 v143, v143\nv_exp_f32 v144, v144\nv_exp_f32 v145, v145\nv_mfma_f32_32x32x16_bf16 v[104:119], v[156:159], v[50:53], v[104:119]\n" \
    "s_cmp_ge_u32 %[st], 6\ns_cbranch_scc0 .Lrt6_%=\n.Lok6_%=:\nv_exp_f32 v146, v146\nv_exp_f32 v147, v147\nv_exp_f32 v148, v148\nv_exp_f32 v149, v149\n" \
    "v_smfmac_f32_32x32x32_bf16 v[56:71], v[240:243], v[136:143], v43\nv_exp_f32 v150, v150\nv_exp_f32 v151, v151\n" \
    "v_exp_f32 v88, v88\nv_exp_f32 v89, v89\nv_smfmac_f32_32x32x32_bf16 v[56:71], v[244:247], v[144:151], v43\nv_exp_f32 v90, v90\nv_exp_f32 v91, v91\n" \
    "v_exp_f32 v92, v92\nv_exp_f32 v93, v93\nv_mfma_f32_32x32x4_2b_bf16 v[120:151], v[250:251], v[54:55], 0\ns_waitcnt lgkmcnt(0)\nv_exp_f32 v94, v94\n" \
    "v_exp_f32 v95, v95\nv_exp_f32 v96, v96\nv_exp_f32 v97, v97\nv_mov_b32 v42, 7\nv_exp_f32 v98, v98\nv_exp_f32 v99, v99\nv_exp_f32 v100, v100\n" \
    "v_exp_f32 v101, v101\nv_smfmac_f32_32x32x32_bf16 v[72:87], v[184:187], v[88:95], v43\nds_write_b128 v5, v[56:59] offset:36864\nv_exp_f32 v102, v102\n" \
    "v_exp_f32 v103, v103\nv_exp_f32 v104, v104\nv_exp_f32 v105, v105\nv_smfmac_f32_32x32x32_bf16 v[72:87], v[188:191], v[96:103], v43\n" \
    "ds_write_b128 v5, v[60:63] offset:37888\nv_exp_f32 v106, v106\nv_exp_f32 v107, v107\nv_exp_f32 v108, v108\nv_exp_f32 v109, v109\n" \
    "v_mfma_f32_32x32x16_bf16 v[120:135], v[160:163], v[50:53], v[120:135]\nds_write_b128 v5, v[64:67] offset:38912\nv_exp_f32 v110, v110\n" \
    "v_exp_f32 v111, v111\nv_exp_f32 v112, v112\nv_exp_f32 v113, v113\nv_mfma_f32_32x32x16_bf16 v[136:151], v[164:167], v[50:53], v[136:151]\n" \
    "ds_write2_b32 %[f0], v42, v42 offset0:0 offset1:4\nv_exp_f32 v114, v114\nv_exp_f32 v115, v115\nv_exp_f32 v116, v116\nv_exp_f32 v117, v117\n" \
    "v_smfmac_f32_32x32x32_bf16 v[72:87], v[192:195], v[104:111], v43\nv_exp_f32 v118, v118\nv_exp_f32 v119, v119\nv_exp_f32 v120, v120\n" \
    "v_exp_f32 v121, v121\nv_smfmac_f32_32x32x32_bf16 v[72:87], v[196:199], v[112:119], v43\nv_exp_f32 v122, v122\nv_exp_f32 v123, v123\n" \
    "v_exp_f32 v124, v124\nv_exp_f32 v125, v125\nv_mfma_f32_32x32x4_2b_bf16 v[88:119], v[252:253], v[54:55], 0\nv_exp_f32 v126, v126\n" \
    "v_exp_f32 v127, v127\nv_exp_f32 v128, v128\nv_exp_f32 v129, v129\nv_exp_f32 v130, v130\nv_exp_f32 v131, v131\nv_exp_f32 v132, v132\n" \
    "v_exp_f32 v133, v133\nv_smfmac_f32_32x32x32_bf16 v[72:87], v[200:203], v[120:127], v43\nv_exp_f32 v134, v134\nv_exp_f32 v135, v135\n" \
    "v_exp_f32 v136, v136\nv_exp_f32 v137, v137\nv_smfmac_f32_32x32x32_bf16 v[72:87], v[204:207], v[128:135], v43\nv_exp_f32 v138, v138\n" \
    "v_exp_f32 v139, v139\nv_exp_f32 v140, v140\nv_exp_f32 v141, v141\nv_mfma_f32_32x32x16_bf16 v[88:103], v[168:171], v[50:53], v[88:103]\n" \
    "v_exp_f32 v142, v142\nv_exp_f32 v143, v143\nv_exp_f32 v144, v144\nv_exp_f32 v145, v145\n" \
    "v_mfma_f32_32x32x16_bf16 v[104:119], v[172:175], v[50:53], v[104:119]\nv_exp_f32 v146, v146\nv_exp_f32 v147, v147\nv_exp_f32 v148, v148\n" \
    "v_exp_f32 v149, v149\nv_smfmac_f32_32x32x32_bf16 v[72:87], v[208:211], v[136:143], v43\nv_exp_f32 v150, v150\nv_exp_f32 v151, v151\n" \
    "v_exp_f32 v88, v88\nv_exp_f32 v89, v89\nv_smfmac_f32_32x32x32_bf16 v[72:87], v[212:215], v[144:151], v43\nv_exp_f32 v90, v90\nv_exp_f32 v91, v91\n" \
    "v_exp_f32 v92, v92\nv_exp_f32 v93, v93\nv_mfma_f32_32x32x4_2b_bf16 v[120:151], v[254:255], v[54:55], 0\nv_exp_f32 v94, v94\nv_exp_f32 v95, v95\n" \
    "v_exp_f32 v96, v96\nv_exp_f32 v97, v97\nv_exp_f32 v98, v98\nv_exp_f32 v99, v99\nv_exp_f32 v100, v100\nv_exp_f32 v101, v101\n" \
    "v_smfmac_f32_32x32x32_bf16 v[72:87], v[216:219], v[88:95], v43\nv_exp_f32 v102, v102\nv_exp_f32 v103, v103\nv_exp_f32 v104, v104\n" \
    "v_exp_f32 v105, v105\nv_smfmac_f32_32x32x32_bf16 v[72:87], v[220:223], v[96:103], v43\nv_exp_f32 v106, v106\nv_exp_f32 v107, v107\n" \
    "v_exp_f32 v108, v108\nv_exp_f32 v109, v109\nv_mfma_f32_32x32x16_bf16 v[120:135], v[176:179], v[50:53], v[120:135]\nv_exp_f32 v110, v110\n" \
    "v_exp_f32 v111, v111\nv_exp_f32 v112, v112\nv_exp_f32 v113, v113\nv_mfma_f32_32x32x16_bf16 v[136:151], v[180:183], v[50:53], v[136:151]\n" \
    "v_exp_f32 v114, v114\nv_exp_f32 v115, v115\nv_exp_f32 v116, v116\nv_exp_f32 v117, v117\n" \
    "v_smfmac_f32_32x32x32_bf16 v[72:87], v[224:227], v[104:111], v43\nv_exp_f32 v118, v118\nv_exp_f32 v119, v119\nv_exp_f32 v120, v120\n" \
    "v_exp_f32 v121, v121\nv_smfmac_f32_32x32x32_bf16 v[72:87], v[228:231], v[112:119], v43\nds_read_b32 v41, %[f0] offset:12\nv_exp_f32 v122, v122\n" \
    "v_exp_f32 v123, v123\nv_exp_f32 v124, v124\nv_exp_f32 v125, v125\nv_exp_f32 v126, v126\nv_exp_f32 v127, v127\nv_exp_f32 v128, v128\n" \
    "v_exp_f32 v129, v129\nv_exp_f32 v130, v130\nv_exp_f32 v131, v131\nv_exp_f32 v132, v132\nv_exp_f32 v133, v133\n" \
    "v_smfmac_f32_32x32x32_bf16 v[72:87], v[232:235], v[120:127], v43\nds_read_b128 v[56:59], v4 offset:36864\nv_exp_f32 v134, v134\nv_exp_f32 v135, v135\n" \
    "v_exp_f32 v136, v136\nv_exp_f32 v137, v137\nv_smfmac_f32_32x32x32_bf16 v[72:87], v[236:239], v[128:135], v43\nds_read_b128 v[60:63], v4 offset:37888\n" \
    "s_waitcnt lgkmcnt(2)\nv_exp_f32 v138, v138\nv_exp_f32 v139, v139\nv_readfirstlane_b32 %[st], v41\nv_exp_f32 v140, v140\nv_exp_f32 v141, v141\n" \
    "ds_read_b128 v[64:67], v4 offset:38912\nv_exp_f32 v142, v142\nv_exp_f32 v143, v143\nv_exp_f32 v144, v144\nv_exp_f32 v145, v145\n" \
    "s_cmp_ge_u32 %[st], 7\ns_cbranch_scc0 .Lrt7_%=\n.Lok7_%=:\nv_exp_f32 v146, v146\nv_exp_f32 v147, v147\nv_exp_f32 v148, v148\nv_exp_f32 v149, v149\n" \
    "v_smfmac_f32_32x32x32_bf16 v[72:87], v[240:243], v[136:143], v43\nv_exp_f32 v150, v150\nv_exp_f32 v151, v151\ns_nop 1\ns_nop 0\n" \
    "v_smfmac_f32_32x32x32_bf16 v[72:87], v[244:247], v[144:151], v43\ns_waitcnt lgkmcnt(0)\ns_branch .Lend_%=\n.Lrt0_%=:\ns_sleep 2\ns_nop 0\n" \
    "ds_read_b32 v41, %[qd] offset:4\ns_waitcnt lgkmcnt(0)\nv_readfirstlane_b32 %[st], v41\ns_add_i32 %[cap], %[cap], -1\ns_cmp_le_i32 %[cap], 0\n" \
    "s_cbranch_scc1 .Lok0_%=\ns_cmp_ge_u32 %[st], 1\ns_cbranch_scc0 .Lrt0_%=\ns_branch .Lok0_%=\n.Lrt1_%=:\ns_sleep 2\ns_nop 0\n" \
    "ds_read_b32 v41, %[f0] offset:4\nds_read_b128 v[56:59], v3 offset:0\nds_read_b128 v[60:63], v3 offset:1024\nds_read_b128 v[64:67], v3 offset:2048\n" \
    "s_waitcnt lgkmcnt(0)\nv_readfirstlane_b32 %[st], v41\ns_add_i32 %[cap], %[cap], -1\ns_cmp_le_i32 %[cap], 0\ns_cbranch_scc1 .Lok1_%=\n" \
    "s_cmp_ge_u32 %[st], 1\ns_cbranch_scc0 .Lrt1_%=\ns_branch .Lok1_%=\n.Lrt2_%=:\ns_sleep 2\ns_nop 0\nds_read_b32 v41, %[f0] offset:32\n" \
    "ds_read_b128 v[72:75], v5 offset:24576\nds_read_b128 v[76:79], v5 offset:25600\nds_read_b128 v[80:83], v5 offset:26624\ns_waitcnt lgkmcnt(0)\n" \
    "v_readfirstlane_b32 %[st], v41\ns_add_i32 %[cap], %[cap], -1\ns_cmp_le_i32 %[cap], 0\ns_cbranch_scc1 .Lok2_%=\ns_cmp_ge_u32 %[st], 7\n" \
    "s_cbranch_scc0 .Lrt2_%=\ns_branch .Lok2_%=\n.Lrt3_%=:\ns_sleep 2\ns_nop 0\nds_read_b32 v41, %[f0] offset:4\nds_read_b128 v[56:59], v4 offset:0\n" \
    "ds_read_b128 v[60:63], v4 offset:1024\nds_read_b128 v[64:67], v4 offset:2048\ns_waitcnt lgkmcnt(0)\nv_readfirstlane_b32 %[st], v41\n" \
    "s_add_i32 %[cap], %[cap], -1\ns_cmp_le_i32 %[cap], 0\ns_cbranch_scc1 .Lok3_%=\ns_cmp_ge_u32 %[st], 3\ns_cbranch_scc0 .Lrt3_%=\ns_branch .Lok3_%=\n" \
    ".Lrt4_%=:\ns_sleep 2\ns_nop 0\nds_read_b32 v41, %[f0] offset:4\nds_read_b128 v[72:75], v3 offset:12288\nds_read_b128 v[76:79], v3 offset:13312\n" \
    "ds_read_b128 v[80:83], v3 offset:14336\ns_waitcnt lgkmcnt(0)\nv_readfirstlane_b32 %[st], v41\ns_add_i32 %[cap], %[cap], -1\ns_cmp_le_i32 %[cap], 0\n" \
    "s_cbranch_scc1 .Lok4_%=\ns_cmp_ge_u32 %[st], 2\ns_cbranch_scc0 .Lrt4_%=\ns_branch .Lok4_%=\n.Lrt5_%=:\ns_sleep 2\ns_nop 0\n" \
    "ds_read_b32 v41, %[f0] offset:32\nds_read_b128 v[56:59], v5 offset:36864\nds_read_b128 v[60:63], v5 offset:37888\n" \
    "ds_read_b128 v[64:67], v5 offset:38912\ns_waitcnt lgkmcnt(0)\nv_readfirstlane_b32 %[st], v41\ns_add_i32 %[cap], %[cap], -1\ns_cmp_le_i32 %[cap], 0\n" \
    "s_cbranch_scc1 .Lok5_%=\ns_cmp_ge_u32 %[st], 8\ns_cbranch_scc0 .Lrt5_%=\ns_branch .Lok5_%=\n.Lrt6_%=:\ns_sleep 2\ns_nop 0\n" \
    "ds_read_b32 v41, %[f0] offset:4\nds_read_b128 v[72:75], v4 offset:12288\nds_read_b128 v[76:79], v4 offset:13312\n" \
    "ds_read_b128 v[80:83], v4 offset:14336\ns_waitcnt lgkmcnt(0)\nv_readfirstlane_b32 %[st], v41\ns_add_i32 %[cap], %[cap], -1\ns_cmp_le_i32 %[cap], 0\n" \
    "s_cbranch_scc1 .Lok6_%=\ns_cmp_ge_u32 %[st], 6\ns_cbranch_scc0 .Lrt6_%=\ns_branch .Lok6_%=\n.Lrt7_%=:\ns_sleep 2\ns_nop 0\n" \
    "ds_read_b32 v41, %[f0] offset:12\nds_read_b128 v[56:59], v4 offset:36864\nds_read_b128 v[60:63], v4 offset:37888\n" \
    "ds_read_b128 v[64:67], v4 offset:38912\ns_waitcnt lgkmcnt(0)\nv_readfirstlane_b32 %[st], v41\ns_add_i32 %[cap], %[cap], -1\ns_cmp_le_i32 %[cap], 0\n" \
    "s_cbranch_scc1 .Lok7_%=\ns_cmp_ge_u32 %[st], 7\ns_cbranch_scc0 .Lrt7_%=\ns_branch .Lok7_%=\n.Lend_%=:\ns_nop 12\n"
#define CLOB_ATTN "v41", "v42", "v43", "v44", "v45", "v46", "v47", "v48", "v49", "v50", "v51", "v52", "v53", "v54", "v55", "v88", "v89", "v90", "v91", "v92", "v93", "v94", "v95", "v96", "v97", "v98", "v99", "v100", "v101", "v102", "v103", "v104", "v105", "v106", "v107", "v108", "v109", "v110", "v111", "v112", "v113", "v114", "v115", "v116", "v117", "v118", "v119", "v120", "v121", "v122", "v123", "v124", "v125", "v126", "v127", "v128", "v129", "v130", "v131", "v132", "v133", "v134", "v135", "v136", "v137", "v138", "v139", "v140", "v141", "v142", "v143", "v144", "v145", "v146", "v147", "v148", "v149", "v150", "v151"

__device__ __forceinline__ void dma16(const void* sbase, unsigned voff, unsigned ldsaddr) {
  asm volatile("s_mov_b32 m0, %2\n\ts_nop 0\n\tglobal_load_lds_dwordx4 %0, %1" :: "v"(voff), "s"(sbase), "s"(ldsaddr) : "memory");
}
__device__ __forceinline__ void dma16x4(const void* sbase, unsigned voff, unsigned ldsaddr) {
  asm volatile("s_mov_b32 m0, %2\n\ts_nop 0\n\tglobal_load_lds_dwordx4 %0, %1\n\tglobal_load_lds_dwordx4 %0, %1 offset:1024\n\t"
               "global_load_lds_dwordx4 %0, %1 offset:2048\n\tglobal_load_lds_dwordx4 %0, %1 offset:3072"
               :: "v"(voff), "s"(sbase), "s"(ldsaddr) : "memory");
}
#define PF4(base, voff, lds, n) _Pragma("unroll") for (int i_ = 0; i_ < (n); ++i_)                               \
  asm volatile("s_mov_b32 m0, %2\n\ts_nop 0\n\tglobal_load_lds_dword %0, %1" :: "v"(voff), "s"((base) + i_ * 6400), "s"(lds) : "memory")
__device__ __forceinline__ unsigned pk2(float a, float b) {
  f32x2 v = {a, b};
  return __builtin_bit_cast(unsigned, __builtin_convertvector(v, bf16x2));
}
__device__ __forceinline__ float bfr(float a) { return __uint_as_float(pk2(a, 0.f) << 16); }
__device__ __forceinline__ float blo(unsigned u) { return __uint_as_float(u << 16); }
__device__ __forceinline__ float bhi(unsigned u) { return __uint_as_float(u & 0xffff0000u); }
__device__ __forceinline__ float sigm(float z) { return __builtin_amdgcn_rcpf(1.f + __expf(-z)); }
__device__ __forceinline__ s16x4 trd(lchar* p) { return __builtin_amdgcn_ds_read_tr16_b64_v4i16((LAS s16x4*)p); }
__device__ __forceinline__ bf16x8 cat8(s16x4 a, s16x4 b) {
  return __builtin_bit_cast(bf16x8, __builtin_shufflevector(a, b, 0, 1, 2, 3, 4, 5, 6, 7));
}
__device__ __forceinline__ LAS unsigned* miscp(lchar* sm, int i) { return (LAS unsigned*)(sm + L_MISC) + i; }
__device__ __forceinline__ unsigned ldsw(lchar* sm, int i) {
  return __hip_atomic_load(miscp(sm, i), __ATOMIC_RELAXED, __HIP_MEMORY_SCOPE_WORKGROUP);
}
__device__ __forceinline__ void ldss(lchar* sm, int i, unsigned v) {
  asm volatile("" ::: "memory");
  __hip_atomic_store(miscp(sm, i), v, __ATOMIC_RELAXED, __HIP_MEMORY_SCOPE_WORKGROUP);
}
template <int SLP = 1>
__device__ __forceinline__ unsigned lds_wait_ge(lchar* sm, int i, unsigned v, unsigned mask = ~0u) {
  unsigned r = 0;
#pragma unroll 1
  for (unsigned n = 0; n < SPIN_CAP; ++n) {
    r = __builtin_amdgcn_readfirstlane(ldsw(sm, i));
    if ((r & mask) >= v) break;
    if (SLP) __builtin_amdgcn_s_sleep(SLP);
  }
  asm volatile("" ::: "memory");
  return r;
}
__device__ __forceinline__ unsigned lds_arrive(lchar* sm, int i) {
  unsigned r;
  asm volatile("s_mov_b64 exec, 1\n\tds_add_rtn_u32 %0, %1, %2\n\ts_mov_b64 exec, -1\n\ts_waitcnt lgkmcnt(0)"
               : "=&v"(r) : "v"(miscp(sm, i)), "v"(1u) : "memory");
  return __builtin_amdgcn_readfirstlane(r);
}
__device__ __forceinline__ void lds_signal(lchar* sm, int i, unsigned v = 1u) {
  asm volatile("s_mov_b64 exec, 1\n\tds_add_u32 %0, %1\n\ts_mov_b64 exec, -1" :: "v"(miscp(sm, i)), "v"(v) : "memory");
}
__device__ __forceinline__ void lds_setbit(lchar* sm, int i, unsigned bits) {
  asm volatile("s_mov_b64 exec, 1\n\tds_or_b32 %0, %1\n\ts_mov_b64 exec, -1" :: "v"(miscp(sm, i)), "v"(bits) : "memory");
}
template <int CTRL> __device__ __forceinline__ float dppadd(float v) {
  return v + __builtin_bit_cast(float, __builtin_amdgcn_update_dpp(0, __builtin_bit_cast(int, v), CTRL, 0xf, 0xf, true));
}
__device__ __forceinline__ float wsum63(float v) {
  v = dppadd<0x111>(v); v = dppadd<0x112>(v); v = dppadd<0x114>(v); v = dppadd<0x118>(v);
  v = dppadd<0x142>(v); v = dppadd<0x143>(v);
  return v;
}
template <int CTRL> __device__ __forceinline__ unsigned dppmax(unsigned v) {
  return max(v, (unsigned)__builtin_amdgcn_update_dpp(0, (int)v, CTRL, 0xf, 0xf, true));
}
__device__ __forceinline__ unsigned rmax15(unsigned v) {
  v = dppmax<0x111>(v); v = dppmax<0x112>(v); v = dppmax<0x114>(v); v = dppmax<0x118>(v);
  return v;
}
__device__ __forceinline__ unsigned now10ns() { return (unsigned)__builtin_amdgcn_s_memrealtime(); }
__device__ __forceinline__ bf16x8 ld16(rsrc_t rs, unsigned vo, unsigned so) {
  return __builtin_bit_cast(bf16x8, __builtin_amdgcn_raw_buffer_load_b128(rs, vo, so, SC1));
}
__device__ __forceinline__ f32x16 pv(bf16x8 v, f32x32 p, int u, f32x16 o) {
  f32x8 h;
#pragma unroll
  for (int i = 0; i < 8; ++i) h[i] = p[8 * u + i];
  return __builtin_amdgcn_smfmac_f32_32x32x32_bf16(v, __builtin_bit_cast(bf16x16, h), o, 0xDDDDDDDD, 0, 0);
}
__device__ __forceinline__ float rowsum(float o8) {
  return __uint_as_float(__builtin_amdgcn_permlane32_swap(__float_as_uint(o8), __float_as_uint(o8), false, false)[1]);
}
__device__ __forceinline__ void o_addr(int j, int tb, int hg, int lane, unsigned& a0, unsigned& a1) {
  const int tok = (j & 1) * 32 + (lane & 31), hh = j >> 1;
  const unsigned base = WS_O + (unsigned)(tb * 4 + (tok >> 4)) * 20480u + (hg >> 2) * 10240;
  a0 = base + (hg & 3) * 2048 + hh * 512 + (lane >> 5) * 256 + (tok & 15) * 16;
  a1 = lane < 32 ? base + 8192 + ((hg & 3) * 4 + hh) * 128 + (tok & 15) * 8 : 0x80000000u;
}
__device__ __forceinline__ void publish_o(rsrc_t rs, const f32x16& o, float l, unsigned a0, unsigned a1) {
  const float inv = __builtin_amdgcn_rcpf(l);
  unsigned p[6];
#pragma unroll
  for (int i = 0; i < 6; ++i) p[i] = min(pk2(o[2 * i] * inv, o[2 * i + 1] * inv), 0xFFFFFFFEu);
  __builtin_amdgcn_raw_buffer_store_b128((u32x4){p[0], p[1], p[2], p[3]}, rs, a0, 0, SC1);
  __builtin_amdgcn_raw_buffer_store_b64((u32x2){p[4], p[5]}, rs, a1, 0, SC1);
}

__global__ void __launch_bounds__(512, 2)
fused_kernel(const char* x, const char* wq, const char* wk, const char* wv, const char* wo, const char* wr,
             const char* wgate, const char* wup, const char* wdown, const char* gw, char* out, char* ws) {
  extern __shared__ __attribute__((aligned(16))) char smem_[];
  lchar* sm = (lchar*)smem_;
  const unsigned smb = (unsigned)(uintptr_t)sm;
  int tid = threadIdx.x, lane = tid & 63;
  const int w = __builtin_amdgcn_readfirstlane(tid >> 6), w4 = w & 3;
  const int b = blockIdx.x, bi = b >> 3, tb = (b & 7) * 4 + (bi >> 3), hg = bi & 7;
  const int ntkv = hg >> 1, rot = bi % 10;
  const bool isV = hg & 1, isB = w >= 4;
  int l15 = lane & 15, g4 = lane >> 4;
  if (w == 0) ldss(sm, lane, 0u);
  asm volatile("" : "+s"(x), "+s"(wq), "+s"(wk), "+s"(wv), "+s"(wo), "+s"(wr), "+s"(wgate), "+s"(wup), "+s"(wdown),
               "+s"(gw), "+s"(out), "+s"(ws));
  const rsrc_t rs = __builtin_amdgcn_make_buffer_rsrc(ws, 0, WS_END, 0x00020000);
  const rsrc_t rx = __builtin_amdgcn_make_buffer_rsrc((void*)x, 0, 2048 * 1280, 0x00020000);
  if ((unsigned)(w - 4) < 3u) {
    __builtin_amdgcn_s_setprio(3);
    const char* wkv = isV ? wv : wk;
    if (w4 == 2) {
      dma16(wr, lane * 16, smb + L_RG);
      dma16(gw, lane * 16, smb + L_RG + 1280);
      dma16(wkv + 24576, lane * 16, smb + L_WKV + 24576);
      if (lane < 16) {
        dma16(wr + 1024, lane * 16, smb + L_RG + 1024);
        dma16(gw + 1024, lane * 16, smb + L_RG + 2304);
      }
    } else {
      const unsigned xo = (unsigned)(64 * tb + 16 * ntkv + 8 * w4 + (lane >> 3)) * 1280u + ((lane & 7) ^ (g4 + 4 * w4)) * 16;
#pragma unroll 1
      for (int c = 0; c < 10; ++c) dma16(x + c * 128, xo, smb + L_XK + c * 2048 + w4 * 1024);
    }
#pragma unroll 1
    for (int i = w4; i < (w4 == 2 ? 6 : w4 + 1); ++i) dma16x4(wkv + i * 4096, lane * 16, smb + L_WKV + i * 4096);
  } else if (w == 7) {
    asm volatile("s_mov_b32 m0, %2\n\ts_nop 0\n\tglobal_load_lds_dword %0, %1 sc1" :: "v"(0u), "s"(ws + WS_EPOCH), "s"(smb + L_Q + 7 * 256) : "memory");
  }
  f32x32 rk1, rv0, rv1; f32x8 rk2;
  unsigned vo, ko, k2o;
#define SET_KVO() do { ko = lane * 16; k2o = 2048 + lane * 8; vo = 2560 + (lane >> 5) * 320 + (lane & 31) * 16; } while (0)
#define KV_INIT() asm volatile(ASM_VINIT : "=&{v[184:215]}"(rv0), "=&{v[216:247]}"(rv1), "=&{v[248:255]}"(rk2) : [c] "v"((lane & 31) == 20 ? 0x3F803F80u : 0u))
#define KV_LOAD(TXT) do { unsigned st_;                                                                              \
    asm volatile(TXT : "+{v[152:183]}"(rk1), "+{v[184:215]}"(rv0), "+{v[216:247]}"(rv1), "+{v[248:255]}"(rk2),       \
                   [st] "=&s"(st_)                                                                                   \
                 : [rs] "s"(rs), [ko] "v"(ko), [k2o] "v"(k2o), [vo] "v"(vo), [sb] "s"(WS_KV + w * 20480), [rot] "s"(bi) \
                 : "memory", "scc"); } while (0)
#define SET_WB(KOFF) do { const int r0 = 8 * g4 + (l15 >> 2), sw = (g4 & 1) << 2;    \
    wb1 = smb + L_W + (KOFF) + (r0 ^ sw) * 160 + (l15 & 3) * 8;                      \
    wb2 = smb + L_W + (KOFF) + ((r0 + 4) ^ sw) * 160 + (l15 & 3) * 8; } while (0)
  unsigned wb1, wb2;
#define xsw ((l15 >> 1) & 7)
  unsigned wo5[5];
#define SET_WO5() _Pragma("unroll") for (int m = 0; m < 5; ++m) {                        \
    const unsigned ch = 64 * m + lane, pr = (ch * 6554u) >> 16, part = ch - pr * 10;     \
    wo5[m] = (pr ^ ((pr >> 1) & 4)) * 1280 + part * 16 + hg * 160; }
#define WQ_CHUNK(c) do { int cc_ = (c) + rot; cc_ -= cc_ >= 10 ? 10 : 0;                 \
    _Pragma("unroll") for (int i = 0; i < 10; ++i)                                       \
      dma16(wq + cc_ * 81920 + (i / 5) * 40960, wo5[i % 5], smb + L_W + (c) * 10240 + i * 1024); } while (0)

  u32x8 ad8; u32x4 ad4; unsigned f0;
#define SET_AD() do { _Pragma("unroll") for (int i = 0; i < 4; ++i) { const int hh = (w4 + i) & 3;                  \
      ad8[i] = smb + L_RING + (isB ? 0 : 24576) + hh * 3072 + lane * 16;                                            \
      ad8[4 + i] = smb + L_Q + hh * 2560 + lane * 16;                                                               \
      ad4[i] = smb + L_Q + hh * 2560 + 1024 + (lane & 31) * 8; }                                                    \
    f0 = smb + L_MISC + (M_RING + (isB ? 0 : 8) + w4) * 4;                                                          \
    asm volatile("" : "+{v[2:9]}"(ad8), "+{v[10:13]}"(ad4), "+{v14}"(f0)); } while (0)
  unsigned oa0, oa1;
  f32x32 oo;
#define KEEP_B "+{v16}"(oa0), "+{v17}"(oa1),
#define KEEP_A "+{v[16:23]}"(pa),
#define IN_A , [rs] "s"(rs), [ko] "v"(ko), [k2o] "v"(k2o), [vo] "v"(vo), [sb] "s"(WS_KV + w * 20480), [rot] "s"(bi)
#define ATTN(TXT, KEEP, XIN) do { unsigned st; int cap = SPIN_CAP;                                                              \
    asm volatile(TXT                                                                                                 \
                 : "=&{v[56:87]}"(oo), KEEP                                                                            \
                   "+{v[152:183]}"(rk1), "+{v[184:215]}"(rv0), "+{v[216:247]}"(rv1), "+{v[248:255]}"(rk2),           \
                   [st] "=&s"(st), [cap] "+s"(cap)                                                           \
                 :                                                                                                   \
                   "{v[2:9]}"(ad8), "{v[10:13]}"(ad4), [f0] "{v14}"(f0), [qd] "v"(miscp(sm, M_QDONE)) XIN                                              \
                 : "memory", "scc", CLOB_ATTN);                                                                      \
    RELANE(); } while (0)
#define RELANE() do { asm volatile("v_mbcnt_lo_u32_b32 %0, -1, 0\n\tv_mbcnt_hi_u32_b32 %0, -1, %0" : "=v"(lane));   \
    tid = w * 64 + lane; l15 = lane & 15; g4 = lane >> 4; } while (0)

  const int jh = (w4 + 3 - (w >> 2)) & 3;
  const int kh = (w >> 1) & 1;
#define FINISH_TILE() do { const float l = rowsum(o[8]); unsigned fb_ = isB ? 16u : 17u;                                     \
    if (__all(l >= 8.67e-19f && l <= 1.15e18f)) publish_o(rs, o, l, oa0, oa1);                                               \
    else fb_ += 256u << (2 * jh + (w >> 2));                                                                                 \
    asm volatile("s_mov_b64 exec, 1\n\tds_max_u32 %0, %1\n\ts_mov_b64 exec, -1" :: "v"(miscp(sm, M_TPA - (w >> 2))), "v"(now10ns()) : "memory"); \
    lds_signal(sm, M_FIN, fb_); } while (0)
  unsigned so = WS_O + (unsigned)(tb * 4 + (w & 1)) * 20480u + kh * 10240;
  asm volatile("" : "+s"(so));
  u32x8 pa;
#define SET_PA() do { SET_WB(kh * 51200); pa[0] = wb1; pa[1] = wb2; pa[2] = lane * 16; pa[3] = 8192 + g4 * 256 + l15 * 8;    \
    pa[4] = smb + L_RING + kh * 24576 + (16 * (w & 1) + l15) * 336 + g4 * 16; } while (0)
#define W_PRE() f32x32 w1, w2, w3, w4r;                                                                                      \
    asm volatile(ASM_PRE : "=&{v[128:159]}"(w1), "=&{v[160:191]}"(w2), "=&{v[192:223]}"(w3), "=&{v[224:255]}"(w4r)           \
                 : [wb1] "v"(pa[0]), [wb2] "v"(pa[1]) : "memory")
#define PROJ(H) do { int cap = SPIN_CAP; unsigned ret_;                                                                      \
    asm volatile(ASM_GEMM2                                                                                                   \
                 : "+{v[128:159]}"(w1), [cap] "+s"(cap), [ret] "=&s"(ret_)                                          \
                 : "{v[160:191]}"(w2), "{v[192:223]}"(w3), "{v[224:255]}"(w4r), [rs] "s"(rs), [so0] "s"(so + (H) * 40960),   \
                   [so1] "s"(so + (H) * 40960 + 4096), [wb1] "v"(pa[0]), [wb2] "v"(pa[1]), [ob] "v"(pa[2]),             \
                   [ob2] "v"(pa[3]), [ep] "v"(miscp(sm, M_EPA + (H))), [sa] "v"(pa[4] + (H) * 12288)                              \
                 : "memory", "scc", "vcc", CLOB_GEMM2);                                                                      \
    } while (0)
  const int cg = (b & 7) * 3 + bi / 10, kg = bi % 10;
  const bool ffn = bi < 30;
#define FB_ (sm + L_RING + (8 * g4 + (l15 >> 2)) * 128 + (l15 & 3) * 8)
#define FRD(off) do { lchar* fb = FB_;                                                                                     \
      _Pragma("unroll") for (int i = 0; i < 8; ++i)                                                                          \
        wf[i] = cat8(trd(fb + (off) + (i & 3) * 32 + (i >> 2) * 4096), trd(fb + (off) + (i & 3) * 32 + (i >> 2) * 4096 + 512)); \
      __builtin_amdgcn_sched_barrier(0); } while (0)
#define FCOL() ({ float t[4];                                                                                              \
      _Pragma("unroll") for (int i = 0; i < 4; ++i)                                                                          \
        t[i] = __builtin_amdgcn_mfma_f32_16x16x32_bf16(xs[1], wf[4 + i],                                                     \
               __builtin_amdgcn_mfma_f32_16x16x32_bf16(xs[0], wf[i], (f32x4){0.f, 0.f, 0.f, 0.f}, 0, 0, 0), 0, 0, 0)[0];     \
      g4 & 2 ? (g4 & 1 ? t[3] : t[2]) : (g4 & 1 ? t[1] : t[0]); })
  unsigned tag, fail;
  f32x32 mx;
  if (isB) {
    BAR();
    __builtin_amdgcn_s_setprio(3);
    RELANE();
#define SET_QO() _Pragma("unroll") for (int m = 0; m < 5; ++m) {                                                       \
      const int tok = 16 * w4 + l15, qd = 4 * m + g4, hh = (qd * 52) >> 8, dq = qd - 5 * hh, j = 2 * hh + (tok >> 5);   \
      qo[m] = smb + L_Q + j * 1280 + (dq < 4 ? (dq >> 1) * 512 + (tok & 31) * 16 + (dq & 1) * 8 : 1024 + (tok & 31) * 8);     \
      asm volatile("" : "+v"(qo[m])); }
    unsigned qo[5], wl5[5], gkey = 0;
    if (w4 < 3) {
      lchar* xp = sm + L_XK + l15 * 128;
      lchar* kb = sm + L_WKV + (8 * g4 + (l15 >> 2)) * 40 + (l15 & 3) * 8 + w4 * 32;
      lchar* rb = sm + L_RG + (l15 & 1) * 1280 + g4 * 16;
      const int key16 = 16 * ntkv + l15;
      const unsigned ov = 2560 + ntkv * 640 + (g4 >> 1) * 320 + (g4 & 1) * 8 + l15 * 16 + w4 * 256;
      const unsigned ok = w4 ? 2048 + key16 * 8 : (key16 >> 5) * 1024 + (g4 >> 1) * 512 + (key16 & 31) * 16 + (g4 & 1) * 8;
      unsigned so = (w4 == 0 || (isV ? l15 < 4 : lane < 16)) ? (isV ? ov : ok) : 0x80000000u;
      asm volatile("" : "+v"(so));
      if (w4 == 2) SET_WO5();
      WAITV(0);
      lds_signal(sm, M_KVIN);
      lds_wait_ge<0>(sm, M_KVIN, 3u);
      f32x4 a = {0.f, 0.f, 0.f, 0.f};
#define XFR(s) *(LAS bf16x8*)(xp + ((s) >> 1) * 2048 + (((((s) & 1) * 4 + g4) ^ xsw) * 16))
#define KVG(WFR, SWAP) do { bf16x8 xf[20], wf[20];                                                        \
        _Pragma("unroll") for (int s = 0; s < 24; ++s) {                                                  \
          if (s < 20) { xf[s] = XFR(s); wf[s] = WFR; }                                                    \
          if (s >= 4) a = SWAP ? __builtin_amdgcn_mfma_f32_16x16x32_bf16(xf[s - 4], wf[s - 4], a, 0, 0, 0) \
                               : __builtin_amdgcn_mfma_f32_16x16x32_bf16(wf[s - 4], xf[s - 4], a, 0, 0, 0); \
          __builtin_amdgcn_sched_barrier(0); } } while (0)
      if (w4 == 2) KVG(*(LAS bf16x8*)(rb + s * 64), false);
      else if (isV) KVG(cat8(trd(kb + s * 1280), trd(kb + s * 1280 + 160)), true);
      else {
        KVG(cat8(trd(kb + s * 1280), trd(kb + s * 1280 + 160)), false);
        a *= 0.22360679774997896f * 1.4426950408889634f;
      }
      if (w4 < 2) {
        __builtin_amdgcn_raw_buffer_store_b64((u32x2){pk2(a[0], a[1]), pk2(a[2], a[3])}, rs, so, WS_KV + tb * 5120, SC1);
        lds_wait_ge<0>(sm, M_TOK, 1u);
        lds_wait_ge<0>(sm, M_KEY, 1u);
        tag = __builtin_amdgcn_readfirstlane(ldsw(sm, M_TAG));
        const unsigned mykey = ldsw(sm, M_KEY);
        WAITV(0);
        if (lds_arrive(sm, M_KVOUT) == 1) {
          if (lane == 0) __builtin_amdgcn_raw_buffer_store_b64((u32x2){mykey, tag}, rs, WS_FLAG + (tb * 8 + hg) * 128, 0, SC1);
        }
      } else {
        const int t0 = 64 * tb + 16 * ntkv;
        const unsigned rb16 = pk2(a[0], 0.f) & 0xffffu;
        const unsigned key = rmax15(((rb16 ^ ((rb16 & 0x8000u) ? 0xffffu : 0x8000u)) << 16) | (0xffffu - (unsigned)(t0 + l15)));
        if (lane == 15) ldss(sm, M_KEY, key);
        if (isV) {
          lds_wait_ge<0>(sm, M_TOK, 1u);
          if (lane < 16) __builtin_amdgcn_raw_buffer_store_b64((u32x2){__float_as_uint(bfr(sigm(bfr(a[1])))), ldsw(sm, M_TAG)}, rs, WS_G + (t0 + lane) * 8, 0, SC1);
        }
      }
    } else {
      __builtin_amdgcn_s_setprio(0);
      SET_WO5();
      WAITV(0);
      ldss(sm, M_TAG, *(LAS unsigned*)(sm + L_Q + 7 * 256) + 1u);
      ldss(sm, M_TOK, 1u);
    }
    SET_QO();
    SET_KVO();
    KV_INIT();
    unsigned sn = WS_O + (unsigned)(tb * 4 + w4) * 20480u + (hg >> 2) * 10240 + (hg & 3) * 2048 + lane * 16;
    o_addr(2 * jh + 1, tb, hg, lane, oa0, oa1);
    asm volatile("" : "+v"(sn), "+v"(oa0), "+v"(oa1), "+v"(ko), "+v"(k2o), "+v"(vo));
    SET_AD();
    if (w4 < 2) {
#define POLL_KV() _Pragma("unroll 1") for (unsigned n = 0; n < SPIN_CAP; ++n) {                                       \
        asm volatile("" ::: "memory");                                                                                \
        const u32x2 f0 = __builtin_amdgcn_raw_buffer_load_b64(rs, lane * 128, WS_FLAG + w4 * 16384, SC1);             \
        const u32x2 f1 = __builtin_amdgcn_raw_buffer_load_b64(rs, lane * 128, WS_FLAG + w4 * 16384 + 8192, SC1);      \
        gkey = max(f0[0], f1[0]);                                                                                     \
        if (__all(((f0[1] ^ tag) | (f1[1] ^ tag)) == 0)) break; }
      if (w4 == 0) {
        lds_wait_ge(sm, M_ISS, 2u);
        POLL_KV();
        asm volatile("s_mov_b32 m0, %2\n\ts_nop 0\n\tglobal_load_lds_dword %0, %1 sc1"
                                    :: "v"(lane * 128), "s"(ws + WS_KV + rot * 8192), "s"(smb + L_END) : "memory");
        lds_setbit(sm, M_WQ, 2048u);
      }
    } else {
      if (w4 == 3) {
      lds_wait_ge<0>(sm, M_XISS, 4u);
#pragma unroll 1
      for (int c = 0; c < 4; ++c) {
        WQ_CHUNK(c);
        WAITV(20);
        lds_setbit(sm, M_WQ, (1u << c) >> 2);
      }
      }
      lds_wait_ge<0>(sm, M_XT, 4u);
#pragma unroll 1
      for (int c = w4 + 2; c < 10; c += 2) {
        WQ_CHUNK(c);
        if (c >= 8) {
          lds_signal(sm, M_ISS);
          lds_wait_ge<0>(sm, M_KVOUT, 2u);
        }
        if (c == 5) { WAITV(20); lds_setbit(sm, M_WQ, 4u); }
        WAITV(10);
        if (c > 4) lds_setbit(sm, M_WQ, (1u << c) >> 2);
      }
      WAITV(0); lds_setbit(sm, M_WQ, 64u << w4);
    }
    {
      unsigned st; int cap = SPIN_CAP;
      __builtin_amdgcn_s_setprio(3);
      asm volatile(ASM_QFIN : [st] "=&s"(st), [cap] "+s"(cap)
                   : [e0] "v"(smb + L_RING + w4 * 10240 + lane * 16), [e1] "v"(smb + L_RING + (w4 ^ 1) * 10240 + 5120 + lane * 16),
                     [xs] "v"(miscp(sm, M_XIN + 4 + (w4 >> 1))), [qd] "v"(miscp(sm, M_QDONE)), [q0] "v"(qo[0]), [q1] "v"(qo[1]),
                     [q2] "v"(qo[2]), [q3] "v"(qo[3]), [q4] "v"(qo[4])
                   : "memory", "scc", CLOB_QFIN);
    }
    if (w4 < 2) {
#pragma unroll
      for (int m = 0; m < 5; ++m) {
        const unsigned ch = 64 * m + lane, pr = (ch * 6554u) >> 16, part = ch - pr * 10;
        const unsigned kp = pr ^ ((pr >> 1) & 4), co = part * 16 + hg * 160 + (kp & 3) * 1280;
        wo5[m] = (20 * (kp >> 4) + 2 * (kp & 4) + ((kp >> 1) & 4)) * 1280 + co;
        wl5[m] = (kp & 28) * 6400 + 20480 + co;
        asm volatile("" : "+v"(wo5[m]), "+v"(wl5[m]));
      }
    }
    lds_wait_ge<2>(sm, M_AGO, 4u);
    if (w4 == 1) POLL_KV();
    if (w4 == 0) PF4(wo + bi * 25600, min(lane, 49) * 128, smb + L_W, 4);
    if (w4 >= 2 && ffn) {
      const unsigned st = w4 == 2 ? 3072 : 1280, vo8 = (lane >> 3) * st + (lane & 7) * 16;
      const char* s0 = w4 == 2 ? wgate + kg * 196608 + cg * 128 : wdown + cg * 81920 + kg * 128;
#pragma unroll 1
      for (int i = 0; i < 8; ++i) {
        dma16(s0 + i * 8 * st, vo8, smb + L_RING + (w4 == 2 ? 0 : 16384) + i * 1024);
        if (w4 == 2) dma16(wup + kg * 196608 + cg * 128 + i * 24576, vo8, smb + L_RING + 8192 + i * 1024);
      }
    }
    {
      const u32x4 ones = {~0u, ~0u, ~0u, ~0u};
      __builtin_amdgcn_raw_buffer_store_b128(ones, rs, sn, 0, SC1);
      __builtin_amdgcn_raw_buffer_store_b128(ones, rs, sn + 1024, 0, SC1);
      if (lane < 32) __builtin_amdgcn_raw_buffer_store_b128(ones, rs, sn + 8192 - (hg & 3) * 1536, 0, SC1);
    }
    if (w4 < 2) {
      gkey = rmax15(gkey);
      if (l15 == 15) asm volatile("ds_max_u32 %0, %1" :: "v"(miscp(sm, M_SEL)), "v"(gkey) : "memory");
      lds_signal(sm, M_RDYB);
    }
    lds_wait_ge<3>(sm, M_RDYB, 2u);
    tag = __builtin_amdgcn_readfirstlane(ldsw(sm, M_TAG));
    const int sel = (0xffff - (__builtin_amdgcn_readfirstlane(ldsw(sm, M_SEL)) & 0xffffu)) & 2047;
    asm volatile("" : "={v[152:183]}"(rk1));
    KV_LOAD(ASM_KVLOAD1 ASM_KVLOAD2);
    if (b == 0 && tid == 256) __builtin_amdgcn_raw_buffer_store_b32(tag, rs, WS_EPOCH, 0, SC1);
    bf16x8 xs[2], wf[8];
    xs[0] = __builtin_bit_cast(bf16x8, __builtin_amdgcn_raw_buffer_load_b128(rx, g4 * 16, sel * 1280 + kg * 128, 0));
    xs[1] = __builtin_bit_cast(bf16x8, __builtin_amdgcn_raw_buffer_load_b128(rx, g4 * 16 + 64, sel * 1280 + kg * 128, 0));
    WAITV(0);
    if (lds_arrive(sm, M_SN) == 3 && lane == 0)
      __builtin_amdgcn_raw_buffer_store_b64((u32x2){ldsw(sm, M_T0), tag}, rs, WS_FLAG2 + (tb * 8 + hg) * 128, 0, SC1);
    if (w4 < 2) {
#pragma unroll 1
      for (int p = 0; p < 8; ++p) {
        const char* src = wo + (w4 * 320 + p * 40) * 1280;
#pragma unroll
        for (int m = 0; m < 5; ++m) dma16(src, wo5[m], smb + L_W + (w4 * 10 + p) * 5120 + m * 1024);
      }
#pragma unroll
      for (int m = 0; m < 10; ++m) dma16(wo + (w4 * 320 + (m / 5) * 160) * 1280, wl5[m % 5], smb + L_W + (w4 * 10 + 8) * 5120 + m * 1024);
    }
    if (w4 == 2 && ffn) {
      FRD(0);
      const float pg = FCOL();
      FRD(8192);
      const float pu = FCOL();
      __builtin_amdgcn_raw_buffer_store_b128((u32x4){__float_as_uint(pg), tag, __float_as_uint(pu), tag}, rs,
                                             WS_H + (cg * 10 + kg) * 1024 + lane * 16, 0, SC1);
    }
    lds_signal(sm, M_F1);
    lds_wait_ge<8>(sm, M_F1, 4u);
    lds_wait_ge<8>(sm, M_RING + 8 + w4, B_START);
    __builtin_amdgcn_s_setprio(1);
    ATTN(ASM_ATTN_B, KEEP_B, );
    {
      f32x16 o;
#pragma unroll
      for (int i = 0; i < 16; ++i) o[i] = oo[i] + oo[16 + i];
      FINISH_TILE();
    }
    ldss(sm, M_RING + w4, 8u);
    __builtin_amdgcn_s_setprio(3);
    const unsigned outo = (unsigned)(64 * tb) * 1280u + hg * 160;
    unsigned la[3], ga[3];
    int tk[3], ep[3];
    u32x4 xe[3]; u32x2 gq[3];
#pragma unroll
    for (int r = 0; r < 3; ++r) {
      const int it = r < 2 ? (tid & 255) : 256 + lane, et = (it * 6554) >> 16, hf = r < 2 ? r : w4 & 1;
      ep[r] = it - 10 * et; tk[r] = hf * 32 + et;
      la[r] = L_RING + hf * 12288 + et * 336 + ep[r] * 32; ga[r] = outo + tk[r] * 1280 + ep[r] * 16;
      asm volatile("" : "+v"(ga[r]));
      xe[r] = __builtin_amdgcn_raw_buffer_load_b128(rx, ga[r], 0, 0);
    }
#pragma unroll 1
    for (unsigned n = 0; n < SPIN_CAP; ++n) {
      asm volatile("" ::: "memory");
#pragma unroll
      for (int r = 0; r < 3; ++r) gq[r] = __builtin_amdgcn_raw_buffer_load_b64(rs, tk[r] * 8, WS_G + tb * 512, SC1);
      if (__all(((gq[0][1] ^ tag) | (gq[1][1] ^ tag) | (gq[2][1] ^ tag)) == 0)) break;
    }
    float t[3][8];
#pragma unroll
    for (int r = 0; r < 3; ++r)
#pragma unroll
      for (int i = 0; i < 4; ++i) { t[r][2 * i] = blo(xe[r][i]); t[r][2 * i + 1] = bhi(xe[r][i]); }
    if ((sel >> 6) == tb) {
      lds_wait_ge(sm, M_YOK, 3u);
#pragma unroll
      for (int r = 0; r < 3; ++r)
        if (tk[r] == (sel & 63)) {
          gq[r][0] = ldsw(sm, M_GSEL);
#pragma unroll
          for (int i = 0; i < 8; ++i) t[r][i] = bfr(t[r][i] + ((LAS float*)miscp(sm, M_RED + 32))[8 * ep[r] + i]);
        }
    }
#pragma unroll
    for (int r = 0; r < 3; ++r) {
      const float g1 = bfr(1.f - __uint_as_float(gq[r][0]));
#pragma unroll
      for (int i = 0; i < 8; ++i) mx[8 * r + i] = t[r][i] * g1;
      mx[24 + r] = __uint_as_float(gq[r][0]);
    }
    mx[27] = __uint_as_float(la[0]); mx[28] = __uint_as_float(la[2]); mx[29] = __uint_as_float(ga[0]); mx[30] = __uint_as_float(ga[2]);
    mx[31] = __uint_as_float(ga[1]);
    asm volatile("" : "+{v[128:159]}"(mx));
    fail = lds_wait_ge<0>(sm, M_FIN, 0x80u, 0xf0u) >> 8;
  } else {
    unsigned tw;
    if (w == 0) WAITL0();
    {
      const char* p0 = w == 1 ? wq : w == 2 ? (isV ? wv : wk) : w == 3 ? ws + 8192 : wo;
      const char* p1 = w == 1 ? x + (64 * tb) * 1280 : w == 2 ? wr : w == 3 ? ws + WS_END + 4096 : wgate + kg * 196608;
      const char* p2 = w == 1 ? out + (64 * tb) * 1280 : w == 2 ? gw : w == 3 ? wdown + (ffn ? cg * 81920 : 0) : wup + kg * 196608;
      asm volatile("s_load_dword %0, %1, 0\n\ts_load_dword %0, %2, 0\n\ts_load_dword %0, %3, 0" : "=&s"(tw) : "s"(p0), "s"(p1), "s"(p2));
      const char* pc; int clen;
      asm volatile("s_getpc_b64 %0\n.Lpc_%=:\n\ts_mov_b32 %1, .Lfk_hot_end-.Lpc_%=" : "=s"(pc), "=s"(clen));
      pc += min((bi * 4 + w) * 384, max(clen - 384, 0));
      asm volatile("s_load_dword %0, %1, 0\n\ts_load_dword %0, %1, 64\n\ts_load_dword %0, %1, 128\n\ts_load_dword %0, %1, 192\n\t"
                   "s_load_dword %0, %1, 256\n\ts_load_dword %0, %1, 320" : "+s"(tw) : "s"(pc));
    }
    __builtin_amdgcn_s_barrier();
    RELANE();
#define XTILE(t) ((t) == ntkv ? L_XK : L_W + 40960 + ((t) - ((t) > ntkv)) * 20480)
    {
      const unsigned xl = smb + XTILE(w);
      const unsigned xo = (unsigned)(64 * tb + 16 * w + (lane >> 3)) * 1280u + ((lane & 7) ^ g4) * 16;
      if (w != ntkv) {
        int cc = rot;
#pragma unroll 2
        for (int c = 0; c < 10; ++c) {
          dma16(x + cc * 128, xo, xl + cc * 2048);
          dma16(x + cc * 128 + 10240, xo ^ 64, xl + cc * 2048 + 1024);
          cc = cc == 9 ? 0 : cc + 1;
        }
      }
      PF4(wq + (bi * 200 + w * 50) * 128, min(lane, 49) * 128, smb + L_Q + w * 256, 1);
      lds_signal(sm, M_XISS);
    }
    KV_INIT();
    SET_PA();
    o_addr(2 * jh, tb, hg, lane, oa0, oa1);
    pa[5] = oa0; pa[6] = oa1; pa[7] = L_RING + jh * 3072 + lane * 16;
    asm volatile("" : "+{v[16:23]}"(pa));
    SET_WB((w & 1) * 5120);
    SET_KVO();
    asm volatile("" : "+v"(ko), "+v"(k2o), "+v"(vo), "+v"(wb1), "+v"(wb2));
    SET_AD();
    const int co = l15 * 128 + ((((w & 1) * 4 + g4) ^ xsw) * 16);
    lchar* xp0 = sm + XTILE(w) + co;
    lchar* xp1 = sm + XTILE(w ^ 1) + co;
    asm volatile("" : "+v"(xp0), "+v"(xp1));
    asm volatile("s_waitcnt vmcnt(1) lgkmcnt(0)" : "+s"(tw) :: "memory");
    if (w == ntkv) lds_wait_ge<0>(sm, M_KVIN, 3u);
    ldss(sm, M_XIN + w, 1u);
    lds_wait_ge<0>(sm, M_XIN + (w ^ 1), 1u);
    f32x32 xa, xb; f32x16 xc;
    {
      int cc = rot;
#pragma unroll
      for (int c = 0; c < 10; ++c) {
        const f32x4 f0 = *(LAS f32x4*)(xp0 + cc * 2048), f1 = *(LAS f32x4*)(xp1 + cc * 2048);
#pragma unroll
        for (int i = 0; i < 4; ++i) {
          if (c < 4) { xa[8 * c + i] = f0[i]; xa[8 * c + 4 + i] = f1[i]; }
          else if (c < 8) { xb[8 * c - 32 + i] = f0[i]; xb[8 * c - 28 + i] = f1[i]; }
          else { xc[8 * c - 64 + i] = f0[i]; xc[8 * c - 60 + i] = f1[i]; }
        }
        cc = cc == 9 ? 0 : cc + 1;
      }
    }
    lds_signal(sm, M_XT);
    {
      unsigned st; int cap = SPIN_CAP;
      asm volatile(ASM_GEMM1
                   : "=&{v[152:183]}"(rk1), "+{v[184:215]}"(rv0), "+{v[216:247]}"(rv1), "+{v[248:255]}"(rk2), "+{v[2:9]}"(ad8), "+{v[10:13]}"(ad4), "+{v14}"(f0), "+{v[16:23]}"(pa), [wb1] "+v"(wb1), [wb2] "+v"(wb2), [st] "=&s"(st), [cap] "+s"(cap)
                   : "{v[72:103]}"(xa), "{v[104:135]}"(xb), "{v[136:151]}"(xc), [ma] "v"(miscp(sm, M_WQ)),
                     [ex] "v"(smb + L_RING + w * 10240 + lane * 16), [xs] "v"(miscp(sm, M_XIN + 4 + (w >> 1))) IN_A
                   : "memory", "scc", CLOB_GEMM1);
    }
    if (w == 0) ldss(sm, M_T0, now10ns());
    __builtin_amdgcn_s_setprio(2);
    ATTN(ASM_ATTN_A, KEEP_A, IN_A);
    bf16x8 wf[8];
    lds_wait_ge<4>(sm, M_SN, 4u);
    if (w == 3) {
      FRD(16384);
      ldss(sm, M_FR, 1u);
    }
    tag = __builtin_amdgcn_readfirstlane(ldsw(sm, M_TAG));
    if (w == 3 && ffn) {
      bf16x8 xs[2];
      float gt, up;
#pragma unroll 1
      for (unsigned n = 0; n < SPIN_CAP; ++n) {
        asm volatile("" ::: "memory");
        unsigned bad = 0;
        gt = 0.f; up = 0.f;
#pragma unroll
        for (int i = 0; i < 10; ++i) {
          const u32x4 v = __builtin_amdgcn_raw_buffer_load_b128(rs, lane * 16 + (i & 3) * 1024, WS_H + cg * 10240 + (i >> 2) * 4096, SC1);
          gt += __uint_as_float(v[0]); up += __uint_as_float(v[2]); bad |= (v[1] ^ tag) | (v[3] ^ tag);
        }
        if (__all(bad == 0)) break;
        __builtin_amdgcn_s_sleep(16);
      }
      gt = bfr(gt);
      *(LAS unsigned short*)((lchar*)miscp(sm, M_RED) + lane * 2) = (unsigned short)pk2(bfr(gt * sigm(gt)) * bfr(up), 0.f);
      asm volatile("" ::: "memory");
      xs[0] = *(LAS bf16x8*)((lchar*)miscp(sm, M_RED) + g4 * 16);
      xs[1] = *(LAS bf16x8*)((lchar*)miscp(sm, M_RED) + 64 + g4 * 16);
      const float py = FCOL();
      __builtin_amdgcn_raw_buffer_store_b64((u32x2){__float_as_uint(py), tag}, rs, lane * 8, WS_Y + cg * 5120 + kg * 512, SC1);
    }
    if (w == 0) {
      u32x2 f;
#pragma unroll 1
      for (unsigned n = 0; n < SPIN_CAP; ++n) {
        asm volatile("" ::: "memory");
        f = __builtin_amdgcn_raw_buffer_load_b64(rs, (lane & 7) * 128, WS_FLAG2 + tb * 1024, SC1);
        if (__all(f[1] == tag)) break;
        __builtin_amdgcn_s_sleep(16);
      }
      const unsigned d = rmax15(min(max((int)(f[0] - ldsw(sm, M_T0)), 0), 200));
      if (lane == 15) ldss(sm, M_SOK, d + PACE);
    }
    __builtin_amdgcn_s_setprio(0);
    asm volatile("" : "+{v[16:23]}"(pa));
    LAS f32x4* rp = (LAS f32x4*)(sm + pa[7]);
    oa0 = pa[5]; oa1 = pa[6];
    lds_wait_ge<8>(sm, M_WO, 4u);
    W_PRE();
    __builtin_amdgcn_s_setprio(2);
    const unsigned pace = lds_wait_ge<4>(sm, M_SOK, 1u);
    {
      f32x4 own[3];
      lds_wait_ge<12>(sm, M_RING + w4, 4u);
#pragma unroll
      for (int i = 0; i < 3; ++i) own[i] = rp[1536 + i * 64];
      lds_wait_ge<3>(sm, M_RING + w4 + 1, 5u);
      f32x16 o;
#pragma unroll
      for (int i = 0; i < 3; ++i) {
        const f32x4 t = rp[i * 64] + own[i];
        o[4 * i] = t[0]; o[4 * i + 1] = t[1]; o[4 * i + 2] = t[2]; o[4 * i + 3] = t[3];
      }
      FINISH_TILE();
    }
    const unsigned selkey = __builtin_amdgcn_readfirstlane(ldsw(sm, M_SEL));
    const int sel = (0xffff - (selkey & 0xffffu)) & 2047;
    if ((sel >> 6) == tb && w == 0) {
      RELANE();
      float z = 0.f;
#pragma unroll
      for (int i = 0; i < 5; ++i) {
        const unsigned xv = __builtin_amdgcn_raw_buffer_load_b32(rx, i * 256 + lane * 4, sel * 1280, 0);
        const unsigned gv = *(const __attribute__((address_space(1))) unsigned*)(gw + i * 256 + lane * 4);
        const unsigned p = pk2(blo(xv) * blo(gv), bhi(xv) * bhi(gv));
        z += blo(p) + bhi(p);
      }
      z = wsum63(z);
      if (lane == 63) ldss(sm, M_GSEL, __float_as_uint(bfr(sigm(bfr(z)))));
      lds_signal(sm, M_YOK);
    }
    if ((sel >> 6) == tb && (w == 1 || w == 2)) {
      RELANE();
      const int c = tid - 64;
      float y;
#pragma unroll 1
      for (unsigned n = 0; n < SPIN_CAP; ++n) {
        asm volatile("" ::: "memory");
        unsigned bad = 0;
        y = 0.f;
#pragma unroll
        for (int i = 0; i < 24; ++i) {
          const u32x2 v = __builtin_amdgcn_raw_buffer_load_b64(rs, WS_Y + (80 * hg + min(c, 79)) * 8 + i * 5120, 0, SC1);
          y += __uint_as_float(v[0]); bad |= v[1] ^ tag;
        }
        if (__all(bad == 0)) break;
        __builtin_amdgcn_s_sleep(4);
      }
      const unsigned sb16 = selkey >> 16, rb16 = sb16 ^ ((sb16 & 0x8000u) ? 0x8000u : 0xffffu);
      ((LAS float*)miscp(sm, M_RED + 32))[c] = bfr(bfr(y) * bfr(sigm(blo(rb16))));
      lds_signal(sm, M_YOK);
    }
    f32x32 w0, of0; f32x8 of1;
    {
      int cap = (lds_wait_ge<1>(sm, M_FIN, 4u, 0xfu) & 0x5500u) ? 0 : SPIN_CAP;
      const unsigned t0 = __builtin_amdgcn_readfirstlane(ldsw(sm, M_TPA));
      while (now10ns() - t0 < pace) __builtin_amdgcn_s_sleep(1);
      asm volatile(ASM_EVLOAD
                   : "=&{v[96:127]}"(w0), "+{v[128:159]}"(w1), "=&{v[56:87]}"(of0), "=&{v[88:95]}"(of1), [cap] "+s"(cap)
                   : [rs] "s"(rs), [so0] "s"(so), [so1] "s"(so + 4096), [wb1] "v"(pa[0]), [wb2] "v"(pa[1]), [ob] "v"(pa[2]),
                     [ob2] "v"(pa[3]) : "memory", "scc", "vcc", "v35");
    }
    unsigned sa1 = pa[4] + 12288;
    asm volatile("" : "+v"(sa1));
    lds_wait_ge<12>(sm, M_RING + w4, 7u);
    lds_wait_ge<1>(sm, M_RING + w4, 8u);
    {
      int cap = SPIN_CAP; unsigned ret_, t0; u64 tm;
      asm volatile(ASM_PROJ2
                   : "+{v[96:127]}"(w0), "+{v[128:159]}"(w1), "+{v[56:87]}"(of0), "+{v[88:95]}"(of1), [cap] "+s"(cap),
                     [ret] "=&s"(ret_), [fo] "=&s"(fail), [t0] "=&s"(t0), [tm] "=&{s[96:97]}"(tm)
                   : "{v[160:191]}"(w2), "{v[192:223]}"(w3), "{v[224:255]}"(w4r), [rs] "s"(rs), [so0] "s"(so + 40960),
                     [so1] "s"(so + 45056), [wb1] "v"(pa[0]), [wb2] "v"(pa[1]), [ob] "v"(pa[2]), [ob2] "v"(pa[3]),
                     [sa0] "v"(pa[4]), [sa] "v"(sa1), [ep0] "v"(miscp(sm, M_EPA)), [ep] "v"(miscp(sm, M_EPB)),
                     [fin] "v"(miscp(sm, M_FIN)), [pace] "s"(pace)
                   : "memory", "scc", "vcc", CLOB_PROJ2);
    }
    asm volatile("" : "={v[128:159]}"(mx));
  }
  int wj = w;
  asm volatile("" : "+s"(wj));
  {
  const int w = wj, w4 = w & 3, kh = (w >> 1) & 1;
  const bool isB = w >= 4;
  fail = __builtin_amdgcn_readfirstlane(fail);
  RELANE();
  if (__builtin_expect(fail != 0, 0)) {
    asm volatile(".Lfk_hot_end:");
    BAR();
#pragma unroll 1
    while (fail) {
      const int j = __builtin_ctz(fail);
      fail &= fail - 1;
      SET_KVO();
      const bf16x8 q1 = *(LAS bf16x8*)(sm + L_Q + j * 1280 + lane * 16);
      const s16x4 q2 = *(LAS s16x4*)(sm + L_Q + j * 1280 + 1024 + (lane & 31) * 8);
      float mx = -__builtin_inff();
      f32x16 o = (f32x16)0.f;
#pragma unroll 1
      for (int it = 0; it < 8; ++it) {
        const unsigned kb = WS_KV + (4 * w + (it & 3)) * 5120;
        f32x32 t = __builtin_amdgcn_mfma_f32_32x32x4bf16_1k(
            __builtin_bit_cast(s16x4, __builtin_amdgcn_raw_buffer_load_b64(rs, k2o, kb, SC1)), q2, (f32x32)0.f, 0, 0, 0);
        f32x16 s0, s1;
#pragma unroll
        for (int i = 0; i < 16; ++i) { s0[i] = t[i]; s1[i] = t[16 + i]; }
        s0 = __builtin_amdgcn_mfma_f32_32x32x16_bf16(ld16(rs, ko, kb), q1, s0, 0, 0, 0);
        s1 = __builtin_amdgcn_mfma_f32_32x32x16_bf16(ld16(rs, ko + 1024, kb), q1, s1, 0, 0, 0);
        if (it < 4) {
#pragma unroll
          for (int i = 0; i < 16; ++i) mx = fmaxf(mx, fmaxf(s0[i], s1[i]));
          if (it == 3) {
            LAS float* mp = (LAS float*)(sm + L_RING + 12288);
            mp[tid] = mx;
            BAR();
#pragma unroll
            for (int i = 0; i < 16; ++i) mx = fmaxf(mx, mp[i * 32 + (lane & 31)]);
          }
        } else {
#pragma unroll
          for (int i = 0; i < 16; ++i) { t[i] = __builtin_amdgcn_exp2f(s0[i] - mx); t[16 + i] = __builtin_amdgcn_exp2f(s1[i] - mx); }
#pragma unroll
          for (int u = 0; u < 4; ++u) {
            const unsigned c = (lane & 31) == 20 ? 0x3F803F80u : 0u;
            bf16x8 v = __builtin_bit_cast(bf16x8, (u32x4){c, c, c, c});
            if ((lane & 31) < 20) v = ld16(rs, vo + u * 640, kb);
            o = pv(v, t, u, o);
          }
        }
      }
      LAS f32x4* rp = (LAS f32x4*)(sm + L_RING + 12288) + lane;
      if (w) {
#pragma unroll
        for (int i = 0; i < 3; ++i) rp[w * 192 + (w >> 2) * 768 + i * 64] = (f32x4){o[4 * i], o[4 * i + 1], o[4 * i + 2], o[4 * i + 3]};
      }
      BAR();
      if (w == 0) {
#pragma unroll 1
        for (int v = 1; v < 8; ++v)
#pragma unroll
          for (int i = 0; i < 3; ++i) {
            const f32x4 t = rp[v * 192 + (v >> 2) * 768 + i * 64];
            o[4 * i] += t[0]; o[4 * i + 1] += t[1]; o[4 * i + 2] += t[2]; o[4 * i + 3] += t[3];
          }
        o_addr(j, tb, hg, lane, oa0, oa1);
        publish_o(rs, o, rowsum(o[8]), oa0, oa1);
      }
      BAR();
    }
    if (!isB) {
      RELANE();
      SET_PA();
      W_PRE();
#pragma unroll 1
      for (int h = 0; h < 2; ++h)
        PROJ(h);
      asm volatile("" : "={v[128:159]}"(mx));
    }
    RELANE();
  }
  if (isB) {
    const rsrc_t ro = __builtin_amdgcn_make_buffer_rsrc(out, 0, 2048 * 1280, 0x00020000);
    const unsigned la[3] = {__float_as_uint(mx[27]), __float_as_uint(mx[27]) + 12288, __float_as_uint(mx[28])};
    const unsigned ga[3] = {__float_as_uint(mx[29]), __float_as_uint(mx[31]), __float_as_uint(mx[30])};
#define MIX_RD(r) const f32x4 a##r##0 = *(LAS f32x4*)(sm + la[r]) + *(LAS f32x4*)(sm + la[r] + 24576),                       \
                              a##r##1 = *(LAS f32x4*)(sm + la[r] + 16) + *(LAS f32x4*)(sm + la[r] + 24592)
#define MIX_ST(r) do { const float g = mx[24 + (r)];                                                                         \
      const u32x4 ov = {pk2(g * a##r##0[0] + mx[8 * (r) + 0], g * a##r##0[1] + mx[8 * (r) + 1]), pk2(g * a##r##0[2] + mx[8 * (r) + 2], g * a##r##0[3] + mx[8 * (r) + 3]), \
                        pk2(g * a##r##1[0] + mx[8 * (r) + 4], g * a##r##1[1] + mx[8 * (r) + 5]), pk2(g * a##r##1[2] + mx[8 * (r) + 6], g * a##r##1[3] + mx[8 * (r) + 7])}; \
      __builtin_amdgcn_raw_buffer_store_b128(ov, ro, ga[r], 0, 2); } while (0)
#define MIX_HALF(H) do { lds_wait_ge<0>(sm, M_EPA + (H), 4u);                                                                \
      if (w4 == (H)) { MIX_RD(H); MIX_RD(2); MIX_ST(H); MIX_ST(2); } else { MIX_RD(H); MIX_ST(H); } } while (0)
    MIX_HALF(0);
    MIX_HALF(1);
  }
  }
}

extern "C" void kernel_launch(void* const* d_in, const int* in_sizes, int n_in, void* d_out, int out_size,
                              void* d_ws, size_t ws_size, hipStream_t stream) {
  (void)in_sizes; (void)n_in; (void)out_size; (void)ws_size;
  (void)hipFuncSetAttribute((const void*)fused_kernel, hipFuncAttributeMaxDynamicSharedMemorySize, L_END + 256);
  fused_kernel<<<256, 512, L_END + 256, stream>>>(
      (const char*)d_in[0], (const char*)d_in[1], (const char*)d_in[2], (const char*)d_in[3], (const char*)d_in[4],
      (const char*)d_in[6], (const char*)d_in[7], (const char*)d_in[8], (const char*)d_in[9], (const char*)d_in[10],
      (char*)d_out, (char*)d_ws);
}
```
